```python
import torch

def _softmax(x, axis=-1):
    return torch.softmax(x, dim=axis)
def _gelu(x):
    return torch.nn.functional.gelu(x, approximate="tanh")
def _silu(x):
    return torch.nn.functional.silu(x)
def _sigmoid(x):
    return torch.sigmoid(x)
def _layernorm(x, axis=-1, eps=1e-5):
    m = x.mean(dim=axis, keepdim=True)
    v = x.var(dim=axis, keepdim=True, unbiased=False)
    return (x - m) / torch.sqrt(v + eps)
def _scatter_add(tgt, idx, src, axis=0):
    return tgt.clone().index_add_(axis, idx, src)
def _segment_sum(src, seg, n):
    out = torch.zeros((n,) + src.shape[1:], dtype=src.dtype)
    return out.index_add_(0, seg, src)
def _pad(x, pw):
    flat = tuple(v for pair in reversed(pw) for v in pair)
    return torch.nn.functional.pad(x, flat)

kernel_name = 'standalone__paged_block_table_kv_radixattention__sampling'

def setup_inputs():

    _w = (lambda a: (lambda t: t.to(torch.bfloat16) if t.is_floating_point() else t)(torch.as_tensor(a)))
    B, H, Hkv, Dh = 8, 32, 8, 128
    Lk = 4096
    bs = 16
    nb = max(2, Lk // bs)
    P = max(1, nb // 2)
    n_phys = P + B * (nb - P)
    shared = torch.arange(P, dtype=torch.int64)
    priv = P + torch.arange(B * (nb - P), dtype=torch.int64).reshape(B, nb - P)
    bt = torch.cat([torch.tile(shared[None, :], (B, 1)), priv], dim=1)
    ctx = nb * bs - (torch.arange(1, B + 1, dtype=torch.int64) * 2654435761) % max(1, (nb * bs - P * bs) // 2)
    return {
        "q": _w((lambda *s: torch.randn(*s, dtype=torch.bfloat16) * 0.8)(B, H, Dh)),
        "k_cache": _w((lambda *s: torch.randn(*s, dtype=torch.bfloat16) * 0.8)(n_phys, bs, Hkv, Dh)),
        "v_cache": _w((lambda *s: torch.randn(*s, dtype=torch.bfloat16) * 0.05)(n_phys, bs, Hkv, Dh)),
        "block_table": _w(bt.to(torch.int64)),
        "ctx_lens": _w(torch.as_tensor(ctx).to(torch.int64)),
    }

def reference(q, k_cache, v_cache, block_table, ctx_lens):
    B, H, Dh = q.shape
    n_phys, bs, Hkv, _ = k_cache.shape
    nb = block_table.shape[1]
    Lk = nb * bs
    rep = H // Hkv
    k = k_cache[block_table].reshape(B, Lk, Hkv, Dh)
    v = v_cache[block_table].reshape(B, Lk, Hkv, Dh)
    k = (lambda x, n, axis: torch.repeat_interleave(x, n, dim=axis))(k, rep, axis=2)
    v = (lambda x, n, axis: torch.repeat_interleave(x, n, dim=axis))(v, rep, axis=2)
    scores = torch.einsum("bhd,bkhd->bhk", q, k) * (Dh ** -0.5)
    pos = torch.arange(Lk)
    valid = pos[None, :] < ctx_lens[:, None]
    scores = torch.where(valid[:, None, :], scores, -1e9)
    p = _softmax(scores, axis=-1)
    return torch.einsum("bhk,bkhd->bhd", p, v)
```

```cpp
#include <hip/hip_runtime.h>
#include <hip/hip_bf16.h>
#include <stdint.h>

typedef __attribute__((ext_vector_type(8))) __bf16 bf16x8;
typedef __attribute__((ext_vector_type(4))) float f32x4;
typedef __attribute__((ext_vector_type(2))) float f32x2;
typedef __attribute__((ext_vector_type(4))) unsigned u32x4;
typedef __attribute__((ext_vector_type(2))) unsigned u32x2;
typedef __attribute__((ext_vector_type(4))) int i32x4;

#define KV_AUX 0

namespace {
constexpr int B = 8, H = 32, HKV = 8, D = 128, BS = 16, NB = 256, LK = NB * BS;
constexpr int NWG = 32;
constexpr int WAVES = 8, NTHR = 512;
constexpr int BPW = 4;
constexpr int CHUNK = WAVES * BPW;
constexpr int ROWS = 32;
constexpr int BLK_BYTES = BS * HKV * D * 2;
constexpr int TOK_BYTES = HKV * D * 2;
constexpr int QROW = 272;
constexpr int L_O = 0;
constexpr int L_BT = L_O + WAVES * ROWS * D * 4;
constexpr int L_Q = L_BT + B * NB * 4;
constexpr int L_MX = L_Q + ROWS * QROW;
constexpr int L_LS = L_MX + ROWS * WAVES * 4;
constexpr int L_NS = L_LS + ROWS * WAVES * 4;
constexpr int L_END = L_NS + 32;
constexpr int NSLOT = 64, SLOT_F = 132;
constexpr size_t W_PART = 0, W_MASK = (size_t)HKV * ROWS * NSLOT * SLOT_F * 4;

__device__ __forceinline__ float xg_max(float x) {
  auto s = __builtin_amdgcn_permlane16_swap(__float_as_uint(x), __float_as_uint(x), false, false);
  x = fmaxf(__uint_as_float(s[0]), __uint_as_float(s[1]));
  auto t = __builtin_amdgcn_permlane32_swap(__float_as_uint(x), __float_as_uint(x), false, false);
  return fmaxf(__uint_as_float(t[0]), __uint_as_float(t[1]));
}
__device__ __forceinline__ float xg_sum(float x) {
  auto s = __builtin_amdgcn_permlane16_swap(__float_as_uint(x), __float_as_uint(x), false, false);
  x = __uint_as_float(s[0]) + __uint_as_float(s[1]);
  auto t = __builtin_amdgcn_permlane32_swap(__float_as_uint(x), __float_as_uint(x), false, false);
  return __uint_as_float(t[0]) + __uint_as_float(t[1]);
}
__device__ __forceinline__ unsigned pack_bf16(float lo, float hi) {
  typedef __attribute__((ext_vector_type(2))) __bf16 bf16x2;
  bf16x2 v = {(__bf16)lo, (__bf16)hi};
  return __builtin_bit_cast(unsigned, v);
}
__device__ __forceinline__ f32x4 mfma(u32x4 a, u32x4 b, f32x4 c) {
  return __builtin_amdgcn_mfma_f32_16x16x32_bf16(__builtin_bit_cast(bf16x8, a), __builtin_bit_cast(bf16x8, b), c, 0, 0, 0);
}
template <int CTRL> __device__ __forceinline__ int dpp_shr(int x) {
  return __builtin_amdgcn_update_dpp(0, x, CTRL, 0xf, 0xf, true);
}

__global__ void __launch_bounds__(NTHR, 2)
attn_split(const unsigned char* __restrict__ q, const unsigned char* __restrict__ kc, const unsigned char* __restrict__ vc,
           const int* __restrict__ bt, const int* __restrict__ ctx, unsigned char* __restrict__ ws) {
  __shared__ __attribute__((aligned(16))) unsigned char lds[L_END];
  const int tid = threadIdx.x, lane = tid & 63, w = __builtin_amdgcn_readfirstlane(tid >> 6);
  const int g = lane >> 4, r = lane & 15;
  const int kvh = blockIdx.x & 7, wg = blockIdx.x >> 3;

  const i32x4 ba = *reinterpret_cast<const i32x4*>(bt + w * NB + 4 * lane);
  const i32x4 bz = *reinterpret_cast<const i32x4*>(bt + 4 * lane);
  const u32x4 qv = *reinterpret_cast<const u32x4*>(q + ((size_t)(w * H + kvh * 4 + g) * D) * 2 + r * 16);
  const int craw = ctx[lane & 7];
  const int c0 = ctx[r >> 2], c1 = ctx[4 + (r >> 2)];
  {
    const int mm = (ba.x != bz.x) ? 0 : (ba.y != bz.y) ? 1 : (ba.z != bz.z) ? 2 : (ba.w != bz.w) ? 3 : 4;
    const unsigned long long bal = __ballot(mm < 4);
    int ns = NB;
    if (bal) { const int fl = __builtin_ctzll(bal); ns = 4 * fl + __builtin_amdgcn_readlane(mm, fl); }
    *reinterpret_cast<i32x4*>(lds + L_BT + (w * NB + 4 * lane) * 4) = ba;
    *reinterpret_cast<u32x4*>(lds + L_Q + (w * 4 + g) * QROW + r * 16) = qv;
    if (lane == 0) *reinterpret_cast<int*>(lds + L_NS + 4 * w) = ns;
  }
  constexpr float C2 = 0.088388347648318440f * 1.4426950408889634f;
  const int ce = (craw <= 0 || craw > LK) ? LK : craw;
  const int cl[2] = {(c0 <= 0 || c0 > LK) ? LK : c0, (c1 <= 0 || c1 > LK) ? LK : c1};
  const float sc[2] = {c0 <= 0 ? 0.f : C2, c1 <= 0 ? 0.f : C2};
  const int nblk = (ce + BS - 1) >> 4;
  int mb = max(nblk, dpp_shr<0x111>(nblk)); mb = max(mb, dpp_shr<0x112>(mb)); mb = max(mb, dpp_shr<0x114>(mb));
  const int maxblk = __builtin_amdgcn_readlane(mb, 7);
  __syncthreads();
  int NS;
  {
    const i32x4 n0 = *reinterpret_cast<const i32x4*>(lds + L_NS), n1 = *reinterpret_cast<const i32x4*>(lds + L_NS + 16);
    NS = min(min(min(n0.x, n0.y), min(n0.z, n0.w)), min(min(n1.x, n1.y), min(n1.z, n1.w)));
    NS = __builtin_amdgcn_readfirstlane(min(NS, maxblk));
  }
  int inc = (r < 8) ? max(nblk - NS, 0) : 0;
  inc += dpp_shr<0x111>(inc); inc += dpp_shr<0x112>(inc); inc += dpp_shr<0x114>(inc);
  int E[8];
#pragma unroll
  for (int k = 0; k < 8; ++k) E[k] = NS + __builtin_amdgcn_readlane(inc, k);
  const int U = E[7];
  const int nrounds = (U > NWG * CHUNK) ? 2 : 1, sh = (U > NWG * CHUNK) ? 6 : 5;

  float* part = reinterpret_cast<float*>(ws + W_PART);
  {
    const int bb = wg >> 2;
    int lo = NS, hi = E[0];
#pragma unroll
    for (int k = 1; k < 8; ++k) if (bb == k) { lo = E[k - 1]; hi = E[k]; }
    const int a = (lane * U) >> sh, e = ((lane + 1) * U) >> sh;
    const bool on = (lane < (1 << sh)) && a < e && (a < NS || (a < hi && e > lo));
    const unsigned long long pm = __ballot(on);
    if (tid == 0) *reinterpret_cast<unsigned long long*>(ws + W_MASK + (size_t)(kvh * ROWS + wg) * 8) = pm;
  }

  const int voffK = r * TOK_BYTES + g * 16, voffV = g * 4 * TOK_BYTES + r * 16;
  for (int round = 0; round < nrounds; ++round) {
    const int v = wg * nrounds + round;
    const int s0 = (v * U) >> sh, s1 = ((v + 1) * U) >> sh, n = s1 - s0;
    const int ws0 = s0 + ((w * n) >> 3), nw = s0 + (((w + 1) * n) >> 3) - ws0;
    int cmask = (s0 < NS) ? 0xff : 0;
    {
      int lo = NS;
#pragma unroll
      for (int k = 0; k < 8; ++k) { if (s0 < E[k] && s1 > lo) cmask |= 1 << k; lo = E[k]; }
      if (n <= 0) cmask = 0;
    }
    int jt, mt, pt;
    {
      const int t = lane & 3, f = ws0 + t;
      int start = NS, bs = 0;
#pragma unroll
      for (int k = 0; k < 7; ++k) { const bool ge = f >= E[k]; start = ge ? E[k] : start; bs += ge ? 1 : 0; }
      const bool shd = f < NS, ok = t < nw;
      jt = shd ? f : NS + f - start;
      jt = ok ? jt : 0;
      mt = ok ? (shd ? 0xff : (1 << bs)) : 0;
      pt = *reinterpret_cast<const int*>(lds + L_BT + ((shd || !ok ? 0 : bs) * NB + jt) * 4);
    }
    int jb[BPW], mk[BPW];
    u32x4 kr[BPW][4], vr[BPW][4];
    {
      int ph[BPW];
#pragma unroll
      for (int t = 0; t < BPW; ++t) { ph[t] = __builtin_amdgcn_readlane(pt, t); jb[t] = __builtin_amdgcn_readlane(jt, t); mk[t] = __builtin_amdgcn_readlane(mt, t); }
#pragma unroll
      for (int t = 0; t < BPW; ++t) {
        const __amdgpu_buffer_rsrc_t rs = __builtin_amdgcn_make_buffer_rsrc((void*)(kc + (size_t)ph[t] * BLK_BYTES + kvh * 256), 0, mk[t] ? BLK_BYTES - kvh * 256 : 0, 0x00020000);
#pragma unroll
        for (int i = 0; i < 4; ++i) kr[t][i] = __builtin_amdgcn_raw_buffer_load_b128(rs, voffK + 64 * i, 0, KV_AUX);
      }
#pragma unroll
      for (int t = 0; t < BPW; ++t) {
        const __amdgpu_buffer_rsrc_t rs = __builtin_amdgcn_make_buffer_rsrc((void*)(vc + (size_t)ph[t] * BLK_BYTES + kvh * 256), 0, mk[t] ? BLK_BYTES - kvh * 256 : 0, 0x00020000);
#pragma unroll
        for (int i = 0; i < 4; ++i) vr[t][i] = __builtin_amdgcn_raw_buffer_load_b128(rs, voffV + TOK_BYTES * i, 0, KV_AUX);
      }
    }
    const int wmask = mk[0] | mk[1] | mk[2] | mk[3];
    f32x4 s[2][BPW];
    float mloc[2];
#pragma unroll
    for (int rt = 0; rt < 2; ++rt) {
      mloc[rt] = -INFINITY;
      if ((wmask >> (4 * rt)) & 0xf) {
        u32x4 qf[4];
#pragma unroll
        for (int i = 0; i < 4; ++i) qf[i] = *reinterpret_cast<const u32x4*>(lds + L_Q + (16 * rt + r) * QROW + 64 * i + 16 * g);
        float m = -INFINITY;
#pragma unroll
        for (int t = 0; t < BPW; ++t) {
          f32x4 a = {0.f, 0.f, 0.f, 0.f};
#pragma unroll
          for (int i = 0; i < 4; ++i) a = mfma(kr[t][i], qf[i], a);
          const int lim = ((mk[t] >> (4 * rt + (r >> 2))) & 1) ? cl[rt] - jb[t] * BS - 4 * g : 0;
#pragma unroll
          for (int e = 0; e < 4; ++e) { a[e] = (e < lim) ? a[e] * sc[rt] : -INFINITY; m = fmaxf(m, a[e]); }
          s[rt][t] = a;
        }
        mloc[rt] = xg_max(m);
      } else {
#pragma unroll
        for (int t = 0; t < BPW; ++t) s[rt][t] = f32x4{0.f, 0.f, 0.f, 0.f};
      }
      if (g == 0) *reinterpret_cast<float*>(lds + L_MX + ((16 * rt + r) * WAVES + w) * 4) = mloc[rt];
    }
    __syncthreads();
    f32x4 acc[2][8];
    u32x4 pf[2][2];
#pragma unroll
    for (int rt = 0; rt < 2; ++rt) {
      float l = 0.f;
#pragma unroll
      for (int j = 0; j < 8; ++j) acc[rt][j] = f32x4{0.f, 0.f, 0.f, 0.f};
      pf[rt][0] = u32x4{0, 0, 0, 0}; pf[rt][1] = u32x4{0, 0, 0, 0};
      if ((wmask >> (4 * rt)) & 0xf) {
        const f32x4 m0 = *reinterpret_cast<const f32x4*>(lds + L_MX + (16 * rt + r) * WAVES * 4);
        const f32x4 m1 = *reinterpret_cast<const f32x4*>(lds + L_MX + (16 * rt + r) * WAVES * 4 + 16);
        float M = fmaxf(fmaxf(fmaxf(m0.x, m0.y), fmaxf(m0.z, m0.w)), fmaxf(fmaxf(m1.x, m1.y), fmaxf(m1.z, m1.w)));
        M = (M == -INFINITY) ? 0.f : M;
#pragma unroll
        for (int t = 0; t < BPW; ++t) {
          f32x4 p;
#pragma unroll
          for (int e = 0; e < 4; ++e) { p[e] = __builtin_amdgcn_exp2f(s[rt][t][e] - M); l += p[e]; }
          pf[rt][t >> 1][2 * (t & 1)] = pack_bf16(p[0], p[1]);
          pf[rt][t >> 1][2 * (t & 1) + 1] = pack_bf16(p[2], p[3]);
        }
        l = xg_sum(l);
      }
      if (g == 0) *reinterpret_cast<float*>(lds + L_LS + ((16 * rt + r) * WAVES + w) * 4) = l;
    }
#pragma unroll
    for (int pr = 0; pr < 2; ++pr) {
#pragma unroll
      for (int j = 0; j < 8; ++j) {
        const unsigned sel = (j & 1) ? 0x07060302u : 0x05040100u;
        u32x4 bf;
        bf[0] = __builtin_amdgcn_perm(vr[2 * pr][1][j >> 1], vr[2 * pr][0][j >> 1], sel);
        bf[1] = __builtin_amdgcn_perm(vr[2 * pr][3][j >> 1], vr[2 * pr][2][j >> 1], sel);
        bf[2] = __builtin_amdgcn_perm(vr[2 * pr + 1][1][j >> 1], vr[2 * pr + 1][0][j >> 1], sel);
        bf[3] = __builtin_amdgcn_perm(vr[2 * pr + 1][3][j >> 1], vr[2 * pr + 1][2][j >> 1], sel);
        if (wmask & 0x0f) acc[0][j] = mfma(pf[0][pr], bf, acc[0][j]);
        if (wmask & 0xf0) acc[1][j] = mfma(pf[1][pr], bf, acc[1][j]);
      }
    }
#pragma unroll
    for (int rt = 0; rt < 2; ++rt) {
      if ((cmask >> (4 * rt + g)) & 1) {
#pragma unroll
        for (int e = 0; e < 4; ++e) {
          unsigned char* p = lds + L_O + ((w * ROWS + 16 * rt + 4 * g + e) * D + 4 * r) * 4;
          *reinterpret_cast<f32x4*>(p) = f32x4{acc[rt][0][e], acc[rt][1][e], acc[rt][2][e], acc[rt][3][e]};
          *reinterpret_cast<f32x4*>(p + 256) = f32x4{acc[rt][4][e], acc[rt][5][e], acc[rt][6][e], acc[rt][7][e]};
        }
      }
    }
    __syncthreads();
    {
      int rem = cmask;
      for (int k = 0; k < (w >> 1); ++k) rem &= rem - 1;
      while (rem) {
        const int bb = __builtin_ctz(rem);
        const int row = 4 * bb + ((tid & 127) >> 5), p4 = tid & 31;
        f32x4 o = {0.f, 0.f, 0.f, 0.f};
#pragma unroll
        for (int ww = 0; ww < WAVES; ++ww) o += *reinterpret_cast<const f32x4*>(lds + L_O + ((ww * ROWS + row) * D + 4 * p4) * 4);
        float* slot = part + ((size_t)(kvh * ROWS + row) * NSLOT + v) * SLOT_F;
        *reinterpret_cast<f32x4*>(slot + 4 * p4) = o;
        if (p4 == 0) {
          const f32x4 m0 = *reinterpret_cast<const f32x4*>(lds + L_MX + row * WAVES * 4), m1 = *reinterpret_cast<const f32x4*>(lds + L_MX + row * WAVES * 4 + 16);
          const f32x4 l0 = *reinterpret_cast<const f32x4*>(lds + L_LS + row * WAVES * 4), l1 = *reinterpret_cast<const f32x4*>(lds + L_LS + row * WAVES * 4 + 16);
          const float M = fmaxf(fmaxf(fmaxf(m0.x, m0.y), fmaxf(m0.z, m0.w)), fmaxf(fmaxf(m1.x, m1.y), fmaxf(m1.z, m1.w)));
          const float L = ((l0.x + l0.y) + (l0.z + l0.w)) + ((l1.x + l1.y) + (l1.z + l1.w));
          *reinterpret_cast<f32x2*>(slot + D) = f32x2{M, L};
        }
        rem &= rem - 1; rem &= rem - 1; rem &= rem - 1; rem &= rem - 1;
      }
    }
    if (round + 1 < nrounds) __syncthreads();
  }
}

__global__ void __launch_bounds__(64)
attn_combine(const unsigned char* __restrict__ ws, unsigned char* __restrict__ out) {
  const int lane = threadIdx.x, kvh = blockIdx.x & 7, row = blockIdx.x >> 3;
  const unsigned long long pm = *reinterpret_cast<const unsigned long long*>(ws + W_MASK + (size_t)(kvh * ROWS + row) * 8);
  const float* base = reinterpret_cast<const float*>(ws + W_PART) + (size_t)(kvh * ROWS + row) * NSLOT * SLOT_F;
  float m = -INFINITY, l = 0.f;
  if ((pm >> lane) & 1) { const f32x2 ml = *reinterpret_cast<const f32x2*>(base + lane * SLOT_F + D); m = ml.x; l = ml.y; }
  float M = m;
#pragma unroll
  for (int o = 1; o < 64; o <<= 1) M = fmaxf(M, __shfl_xor(M, o));
  const float wt = (l > 0.f) ? __builtin_amdgcn_exp2f(m - M) : 0.f;
  float L = wt * l;
#pragma unroll
  for (int o = 1; o < 64; o <<= 1) L += __shfl_xor(L, o);
  f32x4 o = {0.f, 0.f, 0.f, 0.f};
  const int p4 = lane & 31, hf = lane >> 5;
  unsigned long long rem = pm;
  while (rem) {
    const int v0 = __builtin_ctzll(rem); rem &= rem - 1;
    const int v1 = rem ? __builtin_ctzll(rem) : v0;
    const float w0 = __shfl(wt, v0), w1 = rem ? __shfl(wt, v1) : 0.f;
    rem &= rem - 1;
    const f32x4 x = *reinterpret_cast<const f32x4*>(base + (hf ? v1 : v0) * SLOT_F + 4 * p4);
    o += x * (hf ? w1 : w0);
  }
#pragma unroll
  for (int e = 0; e < 4; ++e) o[e] += __shfl_xor(o[e], 32);
  const float inv = 1.f / L;
  if (lane < 32) {
    const int dim = 8 * (p4 & 15) + 4 * (p4 >> 4);
    u32x2 pk = {pack_bf16(o[0] * inv, o[1] * inv), pack_bf16(o[2] * inv, o[3] * inv)};
    *reinterpret_cast<u32x2*>(out + ((size_t)((row >> 2) * H + kvh * 4 + (row & 3)) * D + dim) * 2) = pk;
  }
}
}

extern "C" void kernel_launch(void* const* d_in, const int* in_sizes, int n_in, void* d_out, int out_size, void* d_ws,
                              size_t ws_size, hipStream_t stream) {
  attn_split<<<dim3(HKV * NWG), dim3(NTHR), 0, stream>>>((const unsigned char*)d_in[0], (const unsigned char*)d_in[1],
                                                          (const unsigned char*)d_in[2], (const int*)d_in[3],
                                                          (const int*)d_in[4], (unsigned char*)d_ws);
  attn_combine<<<dim3(HKV * ROWS), dim3(64), 0, stream>>>((const unsigned char*)d_ws, (unsigned char*)d_out);
}
```

```cpp
#include <hip/hip_runtime.h>
#include <hip/hip_bf16.h>
#include <stdint.h>

typedef __attribute__((ext_vector_type(8))) __bf16 bf16x8;
typedef __attribute__((ext_vector_type(4))) float f32x4;
typedef __attribute__((ext_vector_type(2))) float f32x2;
typedef __attribute__((ext_vector_type(4))) unsigned u32x4;
typedef __attribute__((ext_vector_type(2))) unsigned u32x2;
typedef __attribute__((ext_vector_type(4))) int i32x4;

#define KV_AUX 2

#define STAMP(i)
namespace {
constexpr int B = 8, H = 32, HKV = 8, D = 128, BS = 16, NB = 256, LK = NB * BS;
constexpr int NWG = 32;
constexpr int WAVES = 8, NTHR = 512;
constexpr int BPW = 4;
constexpr int CHUNK = WAVES * BPW;
constexpr int ROWS = 32;
constexpr int BLK_BYTES = BS * HKV * D * 2;
constexpr int TOK_BYTES = HKV * D * 2;
constexpr int QROW = 272;
constexpr int L_O = 0;
constexpr int L_BT = L_O + WAVES * ROWS * D * 4;
constexpr int L_Q = L_BT + B * NB * 4;
constexpr int L_MX = L_Q + ROWS * QROW;
constexpr int L_LS = L_MX + ROWS * WAVES * 4;
constexpr int L_NS = L_LS + ROWS * WAVES * 4;
constexpr int L_END = L_NS + 32;
constexpr int NSLOT = 64, SLOT_F = 132;
constexpr size_t W_PART = 0, W_MASK = (size_t)HKV * ROWS * NSLOT * SLOT_F * 4;

__device__ __forceinline__ float xg_max(float x) {
  auto s = __builtin_amdgcn_permlane16_swap(__float_as_uint(x), __float_as_uint(x), false, false);
  x = fmaxf(__uint_as_float(s[0]), __uint_as_float(s[1]));
  auto t = __builtin_amdgcn_permlane32_swap(__float_as_uint(x), __float_as_uint(x), false, false);
  return fmaxf(__uint_as_float(t[0]), __uint_as_float(t[1]));
}
__device__ __forceinline__ float xg_sum(float x) {
  auto s = __builtin_amdgcn_permlane16_swap(__float_as_uint(x), __float_as_uint(x), false, false);
  x = __uint_as_float(s[0]) + __uint_as_float(s[1]);
  auto t = __builtin_amdgcn_permlane32_swap(__float_as_uint(x), __float_as_uint(x), false, false);
  return __uint_as_float(t[0]) + __uint_as_float(t[1]);
}
__device__ __forceinline__ unsigned pack_bf16(float lo, float hi) {
  typedef __attribute__((ext_vector_type(2))) __bf16 bf16x2;
  bf16x2 v = {(__bf16)lo, (__bf16)hi};
  return __builtin_bit_cast(unsigned, v);
}
__device__ __forceinline__ f32x4 mfma(u32x4 a, u32x4 b, f32x4 c) {
  return __builtin_amdgcn_mfma_f32_16x16x32_bf16(__builtin_bit_cast(bf16x8, a), __builtin_bit_cast(bf16x8, b), c, 0, 0, 0);
}
template <int CTRL> __device__ __forceinline__ int dpp_shr(int x) {
  return __builtin_amdgcn_update_dpp(0, x, CTRL, 0xf, 0xf, true);
}

__global__ void __launch_bounds__(NTHR, 2)
attn_split(const unsigned char* __restrict__ q, const unsigned char* __restrict__ kc, const unsigned char* __restrict__ vc,
           const int* __restrict__ bt, const int* __restrict__ ctx, unsigned char* __restrict__ ws) {
  __shared__ __attribute__((aligned(16))) unsigned char lds[L_END];
  const int tid = threadIdx.x, lane = tid & 63, w = __builtin_amdgcn_readfirstlane(tid >> 6);
  const int g = lane >> 4, r = lane & 15;
  const int kvh = blockIdx.x & 7, wg = blockIdx.x >> 3;
  STAMP(0);
  const i32x4 ba = *reinterpret_cast<const i32x4*>(bt + w * NB + 4 * lane);
  const i32x4 bz = *reinterpret_cast<const i32x4*>(bt + 4 * lane);
  const u32x4 qv = *reinterpret_cast<const u32x4*>(q + ((size_t)(w * H + kvh * 4 + g) * D) * 2 + r * 16);
  const int craw = ctx[lane & 7];
  const int c0 = ctx[r >> 2], c1 = ctx[4 + (r >> 2)];
  {
    const int mm = (ba.x != bz.x) ? 0 : (ba.y != bz.y) ? 1 : (ba.z != bz.z) ? 2 : (ba.w != bz.w) ? 3 : 4;
    const unsigned long long bal = __ballot(mm < 4);
    int ns = NB;
    if (bal) { const int fl = __builtin_ctzll(bal); ns = 4 * fl + __builtin_amdgcn_readlane(mm, fl); }
    *reinterpret_cast<i32x4*>(lds + L_BT + (w * NB + 4 * lane) * 4) = ba;
    *reinterpret_cast<u32x4*>(lds + L_Q + (w * 4 + g) * QROW + r * 16) = qv;
    if (lane == 0) *reinterpret_cast<int*>(lds + L_NS + 4 * w) = ns;
  }
  constexpr float C2 = 0.088388347648318440f * 1.4426950408889634f;
  const int ce = (craw <= 0 || craw > LK) ? LK : craw;
  const int cl[2] = {(c0 <= 0 || c0 > LK) ? LK : c0, (c1 <= 0 || c1 > LK) ? LK : c1};
  const float sc[2] = {c0 <= 0 ? 0.f : C2, c1 <= 0 ? 0.f : C2};
  const int nblk = (ce + BS - 1) >> 4;
  int mb = max(nblk, dpp_shr<0x111>(nblk)); mb = max(mb, dpp_shr<0x112>(mb)); mb = max(mb, dpp_shr<0x114>(mb));
  const int maxblk = __builtin_amdgcn_readlane(mb, 7);
  __syncthreads();
  STAMP(1);
  int NS;
  {
    const i32x4 n0 = *reinterpret_cast<const i32x4*>(lds + L_NS), n1 = *reinterpret_cast<const i32x4*>(lds + L_NS + 16);
    NS = min(min(min(n0.x, n0.y), min(n0.z, n0.w)), min(min(n1.x, n1.y), min(n1.z, n1.w)));
    NS = __builtin_amdgcn_readfirstlane(min(NS, maxblk));
  }
  int inc = (r < 8) ? max(nblk - NS, 0) : 0;
  inc += dpp_shr<0x111>(inc); inc += dpp_shr<0x112>(inc); inc += dpp_shr<0x114>(inc);
  int E[8];
#pragma unroll
  for (int k = 0; k < 8; ++k) E[k] = NS + __builtin_amdgcn_readlane(inc, k);
  const int U = E[7];
  const int nrounds = (U > NWG * CHUNK) ? 2 : 1, sh = (U > NWG * CHUNK) ? 6 : 5;

  float* part = reinterpret_cast<float*>(ws + W_PART);
  {
    const int bb = wg >> 2;
    int lo = NS, hi = E[0];
#pragma unroll
    for (int k = 1; k < 8; ++k) if (bb == k) { lo = E[k - 1]; hi = E[k]; }
    const int a = (lane * U) >> sh, e = ((lane + 1) * U) >> sh;
    const bool on = (lane < (1 << sh)) && a < e && (a < NS || (a < hi && e > lo));
    const unsigned long long pm = __ballot(on);
    if (tid == 0) *reinterpret_cast<unsigned long long*>(ws + W_MASK + (size_t)(kvh * ROWS + wg) * 8) = pm;
  }

  const int voffK = r * TOK_BYTES + g * 16, voffV = g * 4 * TOK_BYTES + r * 16;
  for (int round = 0; round < nrounds; ++round) {
    const int v = wg * nrounds + round;
    const int s0 = (v * U) >> sh, s1 = ((v + 1) * U) >> sh, n = s1 - s0;
    const int ws0 = s0 + ((w * n) >> 3), nw = s0 + (((w + 1) * n) >> 3) - ws0;
    int cmask = (s0 < NS) ? 0xff : 0;
    {
      int lo = NS;
#pragma unroll
      for (int k = 0; k < 8; ++k) { if (s0 < E[k] && s1 > lo) cmask |= 1 << k; lo = E[k]; }
      if (n <= 0) cmask = 0;
    }
    int jt, mt, pt;
    {
      const int t = lane & 3, f = ws0 + t;
      int start = NS, bs = 0;
#pragma unroll
      for (int k = 0; k < 7; ++k) { const bool ge = f >= E[k]; start = ge ? E[k] : start; bs += ge ? 1 : 0; }
      const bool shd = f < NS, ok = t < nw;
      jt = shd ? f : NS + f - start;
      jt = ok ? jt : 0;
      mt = ok ? (shd ? 0xff : (1 << bs)) : 0;
      pt = *reinterpret_cast<const int*>(lds + L_BT + ((shd || !ok ? 0 : bs) * NB + jt) * 4);
    }
    int jb[BPW], mk[BPW];
    u32x4 kr[BPW][4], vr[BPW][4];
    {
      int ph[BPW];
#pragma unroll
      for (int t = 0; t < BPW; ++t) { ph[t] = __builtin_amdgcn_readlane(pt, t); jb[t] = __builtin_amdgcn_readlane(jt, t); mk[t] = __builtin_amdgcn_readlane(mt, t); }
#pragma unroll
      for (int t = 0; t < BPW; ++t) {
        const __amdgpu_buffer_rsrc_t rs = __builtin_amdgcn_make_buffer_rsrc((void*)(kc + (size_t)ph[t] * BLK_BYTES + kvh * 256), 0, mk[t] ? BLK_BYTES - kvh * 256 : 0, 0x00020000);
#pragma unroll
        for (int i = 0; i < 4; ++i) kr[t][i] = __builtin_amdgcn_raw_buffer_load_b128(rs, voffK + 64 * i, 0, KV_AUX);
      }
#pragma unroll
      for (int t = 0; t < BPW; ++t) {
        const __amdgpu_buffer_rsrc_t rs = __builtin_amdgcn_make_buffer_rsrc((void*)(vc + (size_t)ph[t] * BLK_BYTES + kvh * 256), 0, mk[t] ? BLK_BYTES - kvh * 256 : 0, 0x00020000);
#pragma unroll
        for (int i = 0; i < 4; ++i) vr[t][i] = __builtin_amdgcn_raw_buffer_load_b128(rs, voffV + TOK_BYTES * i, 0, KV_AUX);
      }
    }
    STAMP(2);
    const int wmask = mk[0] | mk[1] | mk[2] | mk[3];
    f32x4 s[2][BPW];
    float mloc[2];
#pragma unroll
    for (int rt = 0; rt < 2; ++rt) {
      mloc[rt] = -INFINITY;
      if ((wmask >> (4 * rt)) & 0xf) {
        u32x4 qf[4];
#pragma unroll
        for (int i = 0; i < 4; ++i) qf[i] = *reinterpret_cast<const u32x4*>(lds + L_Q + (16 * rt + r) * QROW + 64 * i + 16 * g);
        float m = -INFINITY;
#pragma unroll
        for (int t = 0; t < BPW; ++t) {
          f32x4 a = {0.f, 0.f, 0.f, 0.f};
#pragma unroll
          for (int i = 0; i < 4; ++i) a = mfma(kr[t][i], qf[i], a);
          const int lim = ((mk[t] >> (4 * rt + (r >> 2))) & 1) ? cl[rt] - jb[t] * BS - 4 * g : 0;
#pragma unroll
          for (int e = 0; e < 4; ++e) { a[e] = (e < lim) ? a[e] * sc[rt] : -INFINITY; m = fmaxf(m, a[e]); }
          s[rt][t] = a;
        }
        mloc[rt] = xg_max(m);
      } else {
#pragma unroll
        for (int t = 0; t < BPW; ++t) s[rt][t] = f32x4{0.f, 0.f, 0.f, 0.f};
      }
      if (g == 0) *reinterpret_cast<float*>(lds + L_MX + ((16 * rt + r) * WAVES + w) * 4) = mloc[rt];
    }
    STAMP(3);
    __syncthreads();
    STAMP(4);
    f32x4 acc[2][8];
    u32x4 pf[2][2];
#pragma unroll
    for (int rt = 0; rt < 2; ++rt) {
      float l = 0.f;
#pragma unroll
      for (int j = 0; j < 8; ++j) acc[rt][j] = f32x4{0.f, 0.f, 0.f, 0.f};
      pf[rt][0] = u32x4{0, 0, 0, 0}; pf[rt][1] = u32x4{0, 0, 0, 0};
      if ((wmask >> (4 * rt)) & 0xf) {
        const f32x4 m0 = *reinterpret_cast<const f32x4*>(lds + L_MX + (16 * rt + r) * WAVES * 4);
        const f32x4 m1 = *reinterpret_cast<const f32x4*>(lds + L_MX + (16 * rt + r) * WAVES * 4 + 16);
        float M = fmaxf(fmaxf(fmaxf(m0.x, m0.y), fmaxf(m0.z, m0.w)), fmaxf(fmaxf(m1.x, m1.y), fmaxf(m1.z, m1.w)));
        M = (M == -INFINITY) ? 0.f : M;
#pragma unroll
        for (int t = 0; t < BPW; ++t) {
          f32x4 p;
#pragma unroll
          for (int e = 0; e < 4; ++e) { p[e] = __builtin_amdgcn_exp2f(s[rt][t][e] - M); l += p[e]; }
          pf[rt][t >> 1][2 * (t & 1)] = pack_bf16(p[0], p[1]);
          pf[rt][t >> 1][2 * (t & 1) + 1] = pack_bf16(p[2], p[3]);
        }
        l = xg_sum(l);
      }
      if (g == 0) *reinterpret_cast<float*>(lds + L_LS + ((16 * rt + r) * WAVES + w) * 4) = l;
    }
#pragma unroll
    for (int pr = 0; pr < 2; ++pr) {
#pragma unroll
      for (int j = 0; j < 8; ++j) {
        const unsigned sel = (j & 1) ? 0x07060302u : 0x05040100u;
        u32x4 bf;
        bf[0] = __builtin_amdgcn_perm(vr[2 * pr][1][j >> 1], vr[2 * pr][0][j >> 1], sel);
        bf[1] = __builtin_amdgcn_perm(vr[2 * pr][3][j >> 1], vr[2 * pr][2][j >> 1], sel);
        bf[2] = __builtin_amdgcn_perm(vr[2 * pr + 1][1][j >> 1], vr[2 * pr + 1][0][j >> 1], sel);
        bf[3] = __builtin_amdgcn_perm(vr[2 * pr + 1][3][j >> 1], vr[2 * pr + 1][2][j >> 1], sel);
        if (wmask & 0x0f) acc[0][j] = mfma(pf[0][pr], bf, acc[0][j]);
        if (wmask & 0xf0) acc[1][j] = mfma(pf[1][pr], bf, acc[1][j]);
      }
    }
    STAMP(5);
#pragma unroll
    for (int rt = 0; rt < 2; ++rt) {
      if ((cmask >> (4 * rt + g)) & 1) {
#pragma unroll
        for (int e = 0; e < 4; ++e) {
          unsigned char* p = lds + L_O + ((w * ROWS + 16 * rt + 4 * g + e) * D + 4 * r) * 4;
          *reinterpret_cast<f32x4*>(p) = f32x4{acc[rt][0][e], acc[rt][1][e], acc[rt][2][e], acc[rt][3][e]};
          *reinterpret_cast<f32x4*>(p + 256) = f32x4{acc[rt][4][e], acc[rt][5][e], acc[rt][6][e], acc[rt][7][e]};
        }
      }
    }
    __syncthreads();
    STAMP(6);
    {
      int rem = cmask;
      for (int k = 0; k < (w >> 1); ++k) rem &= rem - 1;
      while (rem) {
        const int bb = __builtin_ctz(rem);
        const int row = 4 * bb + ((tid & 127) >> 5), p4 = tid & 31;
        f32x4 o = {0.f, 0.f, 0.f, 0.f};
#pragma unroll
        for (int ww = 0; ww < WAVES; ++ww) o += *reinterpret_cast<const f32x4*>(lds + L_O + ((ww * ROWS + row) * D + 4 * p4) * 4);
        float* slot = part + ((size_t)(kvh * ROWS + row) * NSLOT + v) * SLOT_F;
        *reinterpret_cast<f32x4*>(slot + 4 * p4) = o;
        if (p4 == 0) {
          const f32x4 m0 = *reinterpret_cast<const f32x4*>(lds + L_MX + row * WAVES * 4), m1 = *reinterpret_cast<const f32x4*>(lds + L_MX + row * WAVES * 4 + 16);
          const f32x4 l0 = *reinterpret_cast<const f32x4*>(lds + L_LS + row * WAVES * 4), l1 = *reinterpret_cast<const f32x4*>(lds + L_LS + row * WAVES * 4 + 16);
          const float M = fmaxf(fmaxf(fmaxf(m0.x, m0.y), fmaxf(m0.z, m0.w)), fmaxf(fmaxf(m1.x, m1.y), fmaxf(m1.z, m1.w)));
          const float L = ((l0.x + l0.y) + (l0.z + l0.w)) + ((l1.x + l1.y) + (l1.z + l1.w));
          *reinterpret_cast<f32x2*>(slot + D) = f32x2{M, L};
        }
        rem &= rem - 1; rem &= rem - 1; rem &= rem - 1; rem &= rem - 1;
      }
    }
    if (round + 1 < nrounds) __syncthreads();
  }
}

__global__ void __launch_bounds__(64)
attn_combine(const unsigned char* __restrict__ ws, unsigned char* __restrict__ out) {
  const int lane = threadIdx.x, kvh = blockIdx.x & 7, row = blockIdx.x >> 3;
  const unsigned long long pm = *reinterpret_cast<const unsigned long long*>(ws + W_MASK + (size_t)(kvh * ROWS + row) * 8);
  const float* base = reinterpret_cast<const float*>(ws + W_PART) + (size_t)(kvh * ROWS + row) * NSLOT * SLOT_F;
  float m = -INFINITY, l = 0.f;
  if ((pm >> lane) & 1) { const f32x2 ml = *reinterpret_cast<const f32x2*>(base + lane * SLOT_F + D); m = ml.x; l = ml.y; }
  float M = m;
#pragma unroll
  for (int o = 1; o < 64; o <<= 1) M = fmaxf(M, __shfl_xor(M, o));
  const float wt = (l > 0.f) ? __builtin_amdgcn_exp2f(m - M) : 0.f;
  float L = wt * l;
#pragma unroll
  for (int o = 1; o < 64; o <<= 1) L += __shfl_xor(L, o);
  f32x4 o = {0.f, 0.f, 0.f, 0.f};
  const int p4 = lane & 31, hf = lane >> 5;
  unsigned long long rem = pm;
  while (rem) {
    const int v0 = __builtin_ctzll(rem); rem &= rem - 1;
    const int v1 = rem ? __builtin_ctzll(rem) : v0;
    const float w0 = __shfl(wt, v0), w1 = rem ? __shfl(wt, v1) : 0.f;
    rem &= rem - 1;
    const f32x4 x = *reinterpret_cast<const f32x4*>(base + (hf ? v1 : v0) * SLOT_F + 4 * p4);
    o += x * (hf ? w1 : w0);
  }
#pragma unroll
  for (int e = 0; e < 4; ++e) o[e] += __shfl_xor(o[e], 32);
  const float inv = 1.f / L;
  if (lane < 32) {
    const int dim = 8 * (p4 & 15) + 4 * (p4 >> 4);
    u32x2 pk = {pack_bf16(o[0] * inv, o[1] * inv), pack_bf16(o[2] * inv, o[3] * inv)};
    *reinterpret_cast<u32x2*>(out + ((size_t)((row >> 2) * H + kvh * 4 + (row & 3)) * D + dim) * 2) = pk;
  }
}
}

extern "C" void kernel_launch(void* const* d_in, const int* in_sizes, int n_in, void* d_out, int out_size, void* d_ws,
                              size_t ws_size, hipStream_t stream) {
  attn_split<<<dim3(HKV * NWG), dim3(NTHR), 0, stream>>>((const unsigned char*)d_in[0], (const unsigned char*)d_in[1],
                                                          (const unsigned char*)d_in[2], (const int*)d_in[3],
                                                          (const int*)d_in[4], (unsigned char*)d_ws);
  attn_combine<<<dim3(HKV * ROWS), dim3(64), 0, stream>>>((const unsigned char*)d_ws, (unsigned char*)d_out);
}
```

```cpp
#include <hip/hip_runtime.h>
#include <hip/hip_bf16.h>
#include <stdint.h>

typedef __attribute__((ext_vector_type(8))) __bf16 bf16x8;
typedef __attribute__((ext_vector_type(4))) float f32x4;
typedef __attribute__((ext_vector_type(2))) float f32x2;
typedef __attribute__((ext_vector_type(4))) unsigned u32x4;
typedef __attribute__((ext_vector_type(2))) unsigned u32x2;
typedef __attribute__((ext_vector_type(4))) int i32x4;
#define LAS __attribute__((address_space(3)))

#define K_AUX 2
#define V_AUX 2
#define WNUM 4
#define NSL 38
#define NEARLY 14
#define STAMP(i)

namespace {
constexpr int B = 8, H = 32, HKV = 8, D = 128, BS = 16, NB = 256, LK = NB * BS;
constexpr int NF = (128 - NSL) / 3;
static_assert(NSL + 3 * NF == 128, "four heads share 128 workgroups");
constexpr int WAVES = 8, NTHR = 512;
constexpr int BPW = 4;
constexpr int CHUNK = WAVES * BPW;
constexpr int ROWS = 32;
constexpr int TOK_BYTES = HKV * D * 2;
constexpr int QROW = 272;
constexpr int OROW = 528;
constexpr int WREG = ROWS * OROW;
constexpr int AUX_SC1 = 16;
constexpr int WMUL = (8 * 65536 + 8 + WNUM - 1) / (8 + WNUM);
constexpr int L_O = 0;
constexpr int L_BT = L_O + WAVES * WREG;
constexpr int L_Q = L_BT + B * NB * 4;
constexpr int L_MX = L_Q + ROWS * QROW;
constexpr int L_LS = L_MX + ROWS * WAVES * 4;
constexpr int L_NS = L_LS + ROWS * WAVES * 4;
constexpr int L_END = L_NS + 32;
constexpr int NSLOT = 96, ML_B = 1024, ROW_B = ML_B + NSLOT * 512;
constexpr int W_CELL = 0, W_PART = HKV * ROWS * 64, W_END = W_PART + HKV * ROWS * ROW_B;
constexpr unsigned EMPTY = 0xffffffffu;
constexpr unsigned SPIN_CAP = 1u << 22;
constexpr unsigned rcp20(int n) { return ((1u << 20) + n - 1) / n; }
constexpr int OOB = (int)0x80000000;

__device__ __forceinline__ float fmax3(float a, float b, float c) { float d; asm("v_max3_f32 %0, %1, %2, %3" : "=v"(d) : "v"(a), "v"(b), "v"(c)); return d; }
__device__ __forceinline__ float xg_max(float x) {
  auto s = __builtin_amdgcn_permlane16_swap(__float_as_uint(x), __float_as_uint(x), false, false);
  x = fmaxf(__uint_as_float(s[0]), __uint_as_float(s[1]));
  auto t = __builtin_amdgcn_permlane32_swap(__float_as_uint(x), __float_as_uint(x), false, false);
  return fmaxf(__uint_as_float(t[0]), __uint_as_float(t[1]));
}
__device__ __forceinline__ float xg_sum(float x) {
  auto s = __builtin_amdgcn_permlane16_swap(__float_as_uint(x), __float_as_uint(x), false, false);
  x = __uint_as_float(s[0]) + __uint_as_float(s[1]);
  auto t = __builtin_amdgcn_permlane32_swap(__float_as_uint(x), __float_as_uint(x), false, false);
  return __uint_as_float(t[0]) + __uint_as_float(t[1]);
}
__device__ __forceinline__ float xh_max(float x) {
  auto t = __builtin_amdgcn_permlane32_swap(__float_as_uint(x), __float_as_uint(x), false, false);
  return fmaxf(__uint_as_float(t[0]), __uint_as_float(t[1]));
}
__device__ __forceinline__ float xh_sum(float x) {
  auto t = __builtin_amdgcn_permlane32_swap(__float_as_uint(x), __float_as_uint(x), false, false);
  return __uint_as_float(t[0]) + __uint_as_float(t[1]);
}
__device__ __forceinline__ unsigned pack_bf16(float lo, float hi) {
  typedef __attribute__((ext_vector_type(2))) __bf16 bf16x2;
  bf16x2 v = {(__bf16)lo, (__bf16)hi};
  return __builtin_bit_cast(unsigned, v);
}
__device__ __forceinline__ f32x4 mfma(u32x4 a, u32x4 b, f32x4 c) {
  return __builtin_amdgcn_mfma_f32_16x16x32_bf16(__builtin_bit_cast(bf16x8, a), __builtin_bit_cast(bf16x8, b), c, 0, 0, 0);
}
template <int CTRL> __device__ __forceinline__ int dpp0(int x) {
  return __builtin_amdgcn_update_dpp(0, x, CTRL, 0xf, 0xf, true);
}
__device__ __forceinline__ unsigned fix(float x) { return min(__float_as_uint(x), EMPTY - 1u); }

__global__ void __launch_bounds__(NTHR, 2)
attn_radix(const unsigned char* __restrict__ q, const unsigned char* __restrict__ kc, const unsigned char* __restrict__ vc,
           const int* __restrict__ bt, const int* __restrict__ ctx, unsigned char* __restrict__ out,
           unsigned char* __restrict__ ws, unsigned long long kv_bytes) {
  __shared__ __attribute__((aligned(16))) unsigned char lds[L_END];
  const int tid = threadIdx.x, lane = tid & 63, w = __builtin_amdgcn_readfirstlane(tid >> 6);
  const int g = lane >> 4, r = lane & 15;
  const int ckvh = blockIdx.x & 7, crow = blockIdx.x >> 3;
  STAMP(0);
  asm volatile("" :: "s"(q), "s"(kc), "s"(vc), "s"(bt), "s"(ctx), "s"(out), "s"(ws), "s"(kv_bytes));
  const int ak = (int)((size_t)kc >> 8) & 3, av = (int)((size_t)vc >> 8) & 3;
  const bool skew = ak == av;
  const int slow = (1 - ak) & 3, nsl = skew ? NSL : 32, nf = skew ? NF : 32;
  int kvh, wg, nch;
  {
    const int id = (blockIdx.x & 7) * 32 + (blockIdx.x >> 3), rem = id & 127, t = rem - nsl;
    const int qd = (t * (skew ? (65536 + NF - 1) / NF : 2048)) >> 16;
    kvh = (slow + (id >> 7) * 4 + (t < 0 ? 0 : 1 + qd)) & 7;
    wg = t < 0 ? rem : t - qd * nf;
    nch = t < 0 ? nsl : nf;
  }
  const int cnch = ((ckvh & 3) == slow) ? nsl : nf;
  const i32x4 ba = *reinterpret_cast<const i32x4*>(bt + w * NB + 4 * lane);
  const i32x4 bz = *reinterpret_cast<const i32x4*>(bt + 4 * lane);
  const int craw = ctx[lane & 7];
  const u32x4 qv = *reinterpret_cast<const u32x4*>(q + ((size_t)(w * H + kvh * 4 + g) * D) * 2 + r * 16);
  const int c0 = ctx[r >> 2], c1 = ctx[4 + (r >> 2)];
  const __amdgpu_buffer_rsrc_t rsK = __builtin_amdgcn_make_buffer_rsrc((void*)kc, 0, (unsigned)kv_bytes, 0x00020000);
  const __amdgpu_buffer_rsrc_t rsV = __builtin_amdgcn_make_buffer_rsrc((void*)vc, 0, (unsigned)kv_bytes, 0x00020000);
  const __amdgpu_buffer_rsrc_t rsW = __builtin_amdgcn_make_buffer_rsrc((void*)ws, 0, W_END, 0x00020000);
  const int mycell = W_CELL + (ckvh * ROWS + crow) * 64, myrow = W_PART + (ckvh * ROWS + crow) * ROW_B;
  const u32x4 e4 = {EMPTY, EMPTY, EMPTY, EMPTY};
  u32x2 epoch = {0u, 0u};
  {
    const int m3 = (ba.w != bz.w) ? 3 : 4, m2 = (ba.z != bz.z) ? 2 : m3, m1 = (ba.y != bz.y) ? 1 : m2, mm = (ba.x != bz.x) ? 0 : m1;
    const unsigned long long bal = __ballot(mm < 4);
    const int fl = bal ? __builtin_ctzll(bal) : 63;
    const int ns = 4 * fl + __builtin_amdgcn_readlane(mm, fl);
    *reinterpret_cast<i32x4*>(lds + L_BT + (w * NB + 4 * lane) * 4) = ba;
    if (lane == 0) *reinterpret_cast<int*>(lds + L_NS + 4 * w) = ns;
    *reinterpret_cast<u32x4*>(lds + L_Q + (w * 4 + g) * QROW + r * 16) = qv;
  }
  constexpr float C2 = 0.088388347648318440f * 1.4426950408889634f;
  const int ce = (craw <= 0 || craw > LK) ? LK : craw;
  const int nblk = (ce + BS - 1) >> 4;
  int mb = max(nblk, dpp0<0x111>(nblk)); mb = max(mb, dpp0<0x112>(mb)); mb = max(mb, dpp0<0x114>(mb));
  const int maxblk = __builtin_amdgcn_readlane(mb, 7);
  int cl[2] = {(c0 <= 0 || c0 > LK) ? LK : c0, (c1 <= 0 || c1 > LK) ? LK : c1};
  float sc[2] = {c0 <= 0 ? 0.f : C2, c1 <= 0 ? 0.f : C2};
  const int xr = (r ^ g) * 16;
  const int tag = (r == 8) ? 0xff0000 : ((0x10000 << (r & 7)) | ((r & 7) << 24));
  asm volatile("" : "+v"(cl[0]), "+v"(cl[1]), "+v"(sc[0]), "+v"(sc[1]) :: "memory");
  if (w == WAVES - 1) {
    epoch = __builtin_amdgcn_raw_buffer_load_b64(rsW, mycell, 0, AUX_SC1);
#pragma unroll
    for (int k = 0; k < NEARLY / 2 + 1; ++k) __builtin_amdgcn_raw_buffer_store_b128(e4, rsW, lane * 16 + k * 1024, myrow, AUX_SC1);
  }
  __syncthreads();
  STAMP(1);
  int NS;
  {
    const i32x4 n0 = *reinterpret_cast<const i32x4*>(lds + L_NS), n1 = *reinterpret_cast<const i32x4*>(lds + L_NS + 16);
    NS = min(min(min(n0.x, n0.y), min(n0.z, n0.w)), min(min(n1.x, n1.y), min(n1.z, n1.w)));
    NS = __builtin_amdgcn_readfirstlane(min(NS, maxblk));
  }
  const int len = (r < 8) ? max(nblk - NS, 0) : 0;
  int inc = len + dpp0<0x111>(len); inc += dpp0<0x112>(inc); inc += dpp0<0x114>(inc);
  const int pad = (NS * WNUM) >> 3, NSW = NS + pad;
  const int UW = NSW + __builtin_amdgcn_readlane(inc, 7);
  const int seg_hi = (r == 8) ? NS : NS + inc, seg_lo = (r == 8) ? 0 : NS + inc - len;
  auto rounds_of = [&](int c) { return UW < c * CHUNK ? 1 : UW < 2 * c * CHUNK ? 2 : 3; };
  auto step_of = [&](int c, int rr) {
    const unsigned k1 = c == NSL ? rcp20(NSL) : c == NF ? rcp20(NF) : rcp20(32), k2 = c == NSL ? rcp20(2 * NSL) : c == NF ? rcp20(2 * NF) : rcp20(64),
                   k3 = c == NSL ? rcp20(3 * NSL) : c == NF ? rcp20(3 * NF) : rcp20(96);
    return (unsigned)UW * (rr == 1 ? k1 : rr == 2 ? k2 : k3);
  };
  auto edge = [&](int c, unsigned step) {
    const int x = min((int)((min((unsigned)c, 96u) * step) >> 20), UW);
    return x < NSW ? min((x * WMUL) >> 16, NS) : x - pad;
  };
  auto producers = [&](int b, int lim, unsigned step) {
    const int lo = __builtin_amdgcn_readlane(seg_lo, b), hi = __builtin_amdgcn_readlane(seg_hi, b);
    int cnt = 0;
#pragma unroll
    for (int hh = 0; hh < 2; ++hh) {
      const int c = lane + 64 * hh, a = edge(c, step), e = edge(c + 1, step);
      cnt += __builtin_popcountll(__ballot(c < lim && a < e && (a < NS || (a < hi && e > lo))));
    }
    return cnt;
  };
  const int nrounds = rounds_of(nch);
  const unsigned step = step_of(nch, nrounds);
  int np = 0;

  auto chunk = [&](const int round) __attribute__((always_inline)) {
    const int v = wg * nrounds + round;
    const int s0 = edge(v, step), s1 = edge(v + 1, step), n = s1 - s0;
    const int ws0 = s0 + ((w * n) >> 3), nw = s0 + (((w + 1) * n) >> 3) - ws0;
    int jb[BPW], mk[BPW], so[BPW];
    {
      const int f = ws0 + g;
      const bool in = f >= seg_lo && f < seg_hi && g < nw;
      int pk = in ? (((r == 8) ? f : NS + f - seg_lo) | tag) : 0;
      pk |= dpp0<0x111>(pk); pk |= dpp0<0x112>(pk); pk |= dpp0<0x114>(pk); pk |= dpp0<0x118>(pk);
      const int ph = *reinterpret_cast<const int*>(lds + L_BT + ((pk >> 24) * NB + (pk & 0xffff)) * 4);
      const int sov = pk ? (ph << 15) + kvh * 256 : OOB;
#pragma unroll
      for (int t = 0; t < BPW; ++t) {
        const int p = __builtin_amdgcn_readlane(pk, 16 * t + 15);
        jb[t] = p & 0xffff; mk[t] = (p >> 16) & 0xff;
        so[t] = __builtin_amdgcn_readlane(sov, 16 * t + 15);
      }
    }
    if (w == WAVES - 1 && round == 0) {
      const int cr = rounds_of(cnch);
      np = producers(crow >> 2, cnch * cr, step_of(cnch, cr));
      for (int k = NEARLY; k < np; k += 2)
        if (k + (lane >> 5) < np) __builtin_amdgcn_raw_buffer_store_b128(e4, rsW, lane * 16, myrow + ML_B + k * 512, AUX_SC1);
      asm volatile("s_waitcnt vmcnt(0)" ::: "memory");
      const unsigned long long e1 = (((unsigned long long)epoch.y << 32) | epoch.x) + 1ull;
      epoch = u32x2{(unsigned)e1, (unsigned)(e1 >> 32)};
      if (lane == 0) __builtin_amdgcn_raw_buffer_store_b64(epoch, rsW, mycell + 8, 0, AUX_SC1);
    }
    u32x4 vr[BPW][4];
#pragma unroll
    for (int t = 0; t < BPW; ++t)
#pragma unroll
      for (int i = 0; i < 4; ++i)
        __builtin_amdgcn_raw_ptr_buffer_load_lds(rsK, (LAS void*)(lds + L_O + w * WREG + t * 4096 + i * 1024), 16,
                                                 (4 * i + g) * TOK_BYTES + (xr ^ (64 * i)), so[t], 0, K_AUX);
#pragma unroll
    for (int t = 0; t < BPW; ++t)
#pragma unroll
      for (int i = 0; i < 4; ++i) vr[t][i] = __builtin_amdgcn_raw_buffer_load_b128(rsV, (4 * g + i) * TOK_BYTES + r * 16, so[t], V_AUX);
    STAMP(2);
    const int cmask = (n <= 0) ? 0 : (s0 < NS) ? 0xff : (int)(__ballot(r < 8 && g == 0 && s0 < seg_hi && s1 > seg_lo) & 0xff);
    int tb[2], tr[2];
    {
      int rem = cmask;
      for (int k = 0; k < (w >> 1); ++k) rem &= rem - 1;
#pragma unroll
      for (int it = 0; it < 2; ++it) {
        tb[it] = rem ? __builtin_ctz(rem) : -1;
        tr[it] = producers(tb[it] & 7, v, step);
        rem &= rem - 1; rem &= rem - 1; rem &= rem - 1; rem &= rem - 1;
      }
    }
    const int wmask = mk[0] | mk[1] | mk[2] | mk[3];
    asm volatile("s_waitcnt vmcnt(16)" ::: "memory");
    u32x4 pf[2][2];
#pragma unroll
    for (int rt = 0; rt < 2; ++rt) {
      float m = -INFINITY, l = 0.f;
      pf[rt][0] = u32x4{0, 0, 0, 0}; pf[rt][1] = u32x4{0, 0, 0, 0};
      if ((wmask >> (4 * rt)) & 0xf) {
        u32x4 qf[4];
        f32x4 s[BPW];
#pragma unroll
        for (int i = 0; i < 4; ++i) qf[i] = *reinterpret_cast<const u32x4*>(lds + L_Q + (16 * rt + r) * QROW + 64 * i + 16 * g);
#pragma unroll
        for (int t = 0; t < BPW; ++t) {
          f32x4 a = {0.f, 0.f, 0.f, 0.f};
#pragma unroll
          for (int i = 0; i < 4; ++i)
            a = mfma(*reinterpret_cast<const u32x4*>(lds + L_O + w * WREG + t * 4096 + r * 256 + (xr ^ (64 * i))), qf[i], a);
          const int lim = ((mk[t] >> (4 * rt + (r >> 2))) & 1) ? cl[rt] - jb[t] * BS - 4 * g : 0;
#pragma unroll
          for (int e = 0; e < 4; ++e) { a[e] = (e < lim) ? a[e] * sc[rt] : -INFINITY; m = fmaxf(m, a[e]); }
          s[t] = a;
        }
        m = xg_max(m);
        const float ms = (m == -INFINITY) ? 0.f : m;
#pragma unroll
        for (int t = 0; t < BPW; ++t) {
          f32x4 p;
#pragma unroll
          for (int e = 0; e < 4; ++e) { p[e] = __builtin_amdgcn_exp2f(s[t][e] - ms); l += p[e]; }
          pf[rt][t >> 1][2 * (t & 1)] = pack_bf16(p[0], p[1]);
          pf[rt][t >> 1][2 * (t & 1) + 1] = pack_bf16(p[2], p[3]);
        }
        l = xg_sum(l);
      }
      if (g == 0) {
        *reinterpret_cast<float*>(lds + L_MX + ((16 * rt + r) * WAVES + w) * 4) = m;
        *reinterpret_cast<float*>(lds + L_LS + ((16 * rt + r) * WAVES + w) * 4) = l;
      }
    }
    u32x4 cell = {0u, 0u, 1u, 0u};
    if (w == 0) cell = __builtin_amdgcn_raw_buffer_load_b128(rsW, W_CELL + (kvh * ROWS + (lane & 31)) * 64, 0, AUX_SC1);
    STAMP(3);
    f32x4 acc[2][8];
#pragma unroll
    for (int j = 0; j < 8; ++j) { acc[0][j] = f32x4{0.f, 0.f, 0.f, 0.f}; acc[1][j] = f32x4{0.f, 0.f, 0.f, 0.f}; }
#pragma unroll
    for (int pr = 0; pr < 2; ++pr) {
      u32x4 vt[8];
#pragma unroll
      for (int j = 0; j < 8; ++j) {
        const unsigned sel = (j & 1) ? 0x07060302u : 0x05040100u;
        vt[j][0] = __builtin_amdgcn_perm(vr[2 * pr][1][j >> 1], vr[2 * pr][0][j >> 1], sel);
        vt[j][1] = __builtin_amdgcn_perm(vr[2 * pr][3][j >> 1], vr[2 * pr][2][j >> 1], sel);
        vt[j][2] = __builtin_amdgcn_perm(vr[2 * pr + 1][1][j >> 1], vr[2 * pr + 1][0][j >> 1], sel);
        vt[j][3] = __builtin_amdgcn_perm(vr[2 * pr + 1][3][j >> 1], vr[2 * pr + 1][2][j >> 1], sel);
      }
      if (wmask & 0x0f) {
#pragma unroll
        for (int j = 0; j < 8; ++j) acc[0][j] = mfma(vt[j], pf[0][pr], acc[0][j]);
      }
      if (wmask & 0xf0) {
#pragma unroll
        for (int j = 0; j < 8; ++j) acc[1][j] = mfma(vt[j], pf[1][pr], acc[1][j]);
      }
    }
    STAMP(4);
#pragma unroll
    for (int rt = 0; rt < 2; ++rt) {
      if ((cmask >> (4 * rt + (r >> 2))) & 1) {
#pragma unroll
        for (int j = 0; j < 8; ++j) *reinterpret_cast<f32x4*>(lds + L_O + w * WREG + (16 * rt + r) * OROW + (4 * j + g) * 16) = acc[rt][j];
      }
    }
    if (w == 0) {
      const bool need = (cmask >> ((lane & 31) >> 2)) & 1;
#pragma unroll 1
      for (unsigned spins = 0; spins < SPIN_CAP; ++spins) {
        const unsigned long long e1 = (((unsigned long long)cell.y << 32) | cell.x) + 1ull;
        const bool ok = !need || (cell.z == (unsigned)e1 && cell.w == (unsigned)(e1 >> 32));
        if (__all(ok)) break;
        __builtin_amdgcn_s_sleep(2);
        cell = __builtin_amdgcn_raw_buffer_load_b128(rsW, W_CELL + (kvh * ROWS + (lane & 31)) * 64, 0, AUX_SC1);
      }
    }
    __syncthreads();
    STAMP(5);
#pragma unroll
    for (int it = 0; it < 2; ++it) {
      if (tb[it] >= 0) {
        const int row = 4 * tb[it] + ((tid & 127) >> 5), p4 = tid & 31;
        const f32x4 m0 = *reinterpret_cast<const f32x4*>(lds + L_MX + row * WAVES * 4), m1 = *reinterpret_cast<const f32x4*>(lds + L_MX + row * WAVES * 4 + 16);
        f32x4 l0 = *reinterpret_cast<const f32x4*>(lds + L_LS + row * WAVES * 4), l1 = *reinterpret_cast<const f32x4*>(lds + L_LS + row * WAVES * 4 + 16);
        f32x4 x[WAVES];
#pragma unroll
        for (int ww = 0; ww < WAVES; ++ww) x[ww] = *reinterpret_cast<const f32x4*>(lds + L_O + ww * WREG + row * OROW + 16 * p4);
        const float M = fmax3(fmax3(m0.x, m0.y, m0.z), fmax3(m0.w, m1.x, m1.y), fmax3(m1.z, m1.w, m1.w)), Ms = (M == -INFINITY) ? 0.f : M;
        f32x4 o = {0.f, 0.f, 0.f, 0.f};
        float L = 0.f;
#pragma unroll
        for (int ww = 0; ww < WAVES; ++ww) {
          const float wt = __builtin_amdgcn_exp2f((ww < 4 ? m0[ww & 3] : m1[ww & 3]) - Ms);
          o += x[ww] * wt;
          L = fmaf(wt, ww < 4 ? l0[ww & 3] : l1[ww & 3], L);
        }
        asm volatile("" : "+v"(L));
        const int rbase = W_PART + (kvh * ROWS + row) * ROW_B;
        __builtin_amdgcn_raw_buffer_store_b128(u32x4{fix(o.x), fix(o.y), fix(o.z), fix(o.w)}, rsW, rbase + 16 * p4, ML_B + tr[it] * 512, AUX_SC1);
        if (p4 == 0) __builtin_amdgcn_raw_buffer_store_b64(u32x2{fix(M), fix(L)}, rsW, rbase, tr[it] * 8, AUX_SC1);
      }
    }
  };
  chunk(0);
#pragma unroll 1
  for (int round = 1; round < nrounds; ++round) { __syncthreads(); chunk(round); }
  STAMP(6);
  if (w == WAVES - 1) {
    const int p4 = lane & 31, hf = lane >> 5;
    float M = -INFINITY, L = 0.f;
    f32x4 o = {0.f, 0.f, 0.f, 0.f};
    for (int k0 = 0; k0 < np; k0 += 16) {
      u32x4 x[8]; u32x2 ml[8];
      STAMP(1);
#pragma unroll 1
      for (unsigned spins = 0; spins < SPIN_CAP; ++spins) {
#pragma unroll
        for (int i = 0; i < 8; ++i) {
          const int k = k0 + 2 * i + hf, dead = (np - 1 - k) & OOB;
          x[i] = __builtin_amdgcn_raw_buffer_load_b128(rsW, (ML_B + k * 512 + 16 * p4) | dead, myrow, AUX_SC1);
          ml[i] = __builtin_amdgcn_raw_buffer_load_b64(rsW, (k * 8) | dead, myrow, AUX_SC1);
        }
        unsigned top = 0;
#pragma unroll
        for (int i = 0; i < 8; ++i) top = max(max(max(top, x[i].x), max(x[i].y, x[i].z)), max(max(x[i].w, ml[i].x), ml[i].y));
        if (__all(top != EMPTY)) break;
        __builtin_amdgcn_s_sleep(1);
      }
      STAMP(2);
      float mg = M;
#pragma unroll
      for (int i = 0; i < 8; ++i) mg = fmaxf(mg, (k0 + 2 * i + hf >= np) ? -INFINITY : __uint_as_float(ml[i].x));
      mg = xh_max(mg);
      const float ms = (mg == -INFINITY) ? 0.f : mg;
      const float al = __builtin_amdgcn_exp2f(M - ms);
      o *= al; L *= al; M = mg;
#pragma unroll
      for (int i = 0; i < 8; ++i) {
        const float wt = (k0 + 2 * i + hf >= np) ? 0.f : __builtin_amdgcn_exp2f(__uint_as_float(ml[i].x) - ms);
        o += __builtin_bit_cast(f32x4, x[i]) * wt;
        L = fmaf(wt, __uint_as_float(ml[i].y), L);
      }
    }
#pragma unroll
    for (int e = 0; e < 4; ++e) o[e] = xh_sum(o[e]);
    const float inv = 1.f / xh_sum(L);
    {
      const unsigned pk = pack_bf16((hf ? o[2] : o[0]) * inv, (hf ? o[3] : o[1]) * inv);
      unsigned short* op = reinterpret_cast<unsigned short*>(out) + ((crow >> 2) * H + ckvh * 4 + (crow & 3)) * D + 32 * (p4 & 3) + 16 * hf + (p4 >> 2);
      op[0] = (unsigned short)pk; op[8] = (unsigned short)(pk >> 16);
    }
    if (lane == 0) __builtin_amdgcn_raw_buffer_store_b64(epoch, rsW, mycell, 0, 0);
  }
}
}

extern "C" void kernel_launch(void* const* d_in, const int* in_sizes, int n_in, void* d_out, int out_size, void* d_ws,
                              size_t ws_size, hipStream_t stream) {
  attn_radix<<<dim3(HKV * ROWS), dim3(NTHR), 0, stream>>>((const unsigned char*)d_in[0], (const unsigned char*)d_in[1],
                                                          (const unsigned char*)d_in[2], (const int*)d_in[3],
                                                          (const int*)d_in[4], (unsigned char*)d_out, (unsigned char*)d_ws,
                                                          (unsigned long long)in_sizes[1] * 2ull);
}
```

```cpp
#include <hip/hip_runtime.h>
#include <hip/hip_bf16.h>
#include <stdint.h>

typedef __attribute__((ext_vector_type(8))) __bf16 bf16x8;
typedef __attribute__((ext_vector_type(4))) float f32x4;
typedef __attribute__((ext_vector_type(4))) unsigned u32x4;
typedef __attribute__((ext_vector_type(2))) unsigned u32x2;
typedef __attribute__((ext_vector_type(4))) int i32x4;
typedef __attribute__((ext_vector_type(2))) unsigned short u16x2;
#define LAS __attribute__((address_space(3)))

#define K_AUX 2
#define V_AUX 2
#define WNUM 3
#define NSL 38
#define HOIST 0
#define FASTMASK 1
#define OUT_AUX 0
#define SPREAD 0
#define GAP 3
#define REV 1
#define DPOLL 4
#define PG 7
#define NPOLL 1
#define NEARLY 14
#define STAMP(i)
#define PSTAMP(i)
#define QSTAMP(i)
#define STAMP2(s, i)

namespace {
constexpr int B = 8, H = 32, HKV = 8, D = 128, BS = 16, NB = 256, LK = NB * BS;
constexpr int NF = (128 - NSL) / 3;
static_assert(NSL + 3 * NF == 128, "four heads share 128 workgroups");
constexpr int WAVES = 8, NTHR = 512;
constexpr int BPW = 4;
constexpr int CHUNK = WAVES * BPW;
constexpr int ROWS = 32;
constexpr int TOK_BYTES = HKV * D * 2;
constexpr int QROW = 272;
constexpr int OROW = 528;
constexpr int WREG = ROWS * OROW;
constexpr int AUX_SC1 = 16;
constexpr int WMUL = (8 * 65536 + 8 + WNUM - 1) / (8 + WNUM);
constexpr int L_O = 0;
constexpr int L_BT = L_O + WAVES * WREG;
constexpr int L_Q = L_BT + B * NB * 4;
constexpr int L_MX = L_Q + ROWS * QROW;
constexpr int L_LS = L_MX + ROWS * WAVES * 4;
constexpr int L_NS = L_LS + ROWS * WAVES * 4;
constexpr int L_CNT = L_NS + 16;
constexpr int L_END = L_CNT + 16;
constexpr int NSLOT = 96, ML_B = 1024, SLOT_B = 1024, PAY = ML_B + 512, ROW_B = ML_B + NSLOT * SLOT_B;
constexpr int W_CELL = 0, W_PART = HKV * ROWS * 64, W_END = W_PART + HKV * ROWS * ROW_B;
constexpr unsigned EMPTY = 0xffffffffu;
constexpr unsigned SPIN_CAP = 1u << 22;
constexpr int OOB = (int)0x80000000;
constexpr unsigned rcp32(unsigned n) { return (unsigned)(((1ull << 32) + n - 1) / n); }

__device__ __forceinline__ float fmax3(float a, float b, float c) { float d; asm("v_max3_f32 %0, %1, %2, %3" : "=v"(d) : "v"(a), "v"(b), "v"(c)); return d; }
__device__ __forceinline__ float fmax2(float a, float b) { float d; asm("v_max_f32 %0, %1, %2" : "=v"(d) : "v"(a), "v"(b)); return d; }
__device__ __forceinline__ float xg_max(float x) {
  auto s = __builtin_amdgcn_permlane16_swap(__float_as_uint(x), __float_as_uint(x), false, false);
  x = fmax2(__uint_as_float(s[0]), __uint_as_float(s[1]));
  auto t = __builtin_amdgcn_permlane32_swap(__float_as_uint(x), __float_as_uint(x), false, false);
  return fmax2(__uint_as_float(t[0]), __uint_as_float(t[1]));
}
__device__ __forceinline__ float xg_sum(float x) {
  auto s = __builtin_amdgcn_permlane16_swap(__float_as_uint(x), __float_as_uint(x), false, false);
  x = __uint_as_float(s[0]) + __uint_as_float(s[1]);
  auto t = __builtin_amdgcn_permlane32_swap(__float_as_uint(x), __float_as_uint(x), false, false);
  return __uint_as_float(t[0]) + __uint_as_float(t[1]);
}
__device__ __forceinline__ float xh_max(float x) {
  auto t = __builtin_amdgcn_permlane32_swap(__float_as_uint(x), __float_as_uint(x), false, false);
  return fmaxf(__uint_as_float(t[0]), __uint_as_float(t[1]));
}
__device__ __forceinline__ float xh_sum(float x) {
  auto t = __builtin_amdgcn_permlane32_swap(__float_as_uint(x), __float_as_uint(x), false, false);
  return __uint_as_float(t[0]) + __uint_as_float(t[1]);
}
__device__ __forceinline__ unsigned pack_bf16(float lo, float hi) {
  typedef __attribute__((ext_vector_type(2))) __bf16 bf16x2;
  bf16x2 v = {(__bf16)lo, (__bf16)hi};
  return __builtin_bit_cast(unsigned, v);
}
__device__ __forceinline__ f32x4 mfma(u32x4 a, u32x4 b, f32x4 c) {
  return __builtin_amdgcn_mfma_f32_16x16x32_bf16(__builtin_bit_cast(bf16x8, a), __builtin_bit_cast(bf16x8, b), c, 0, 0, 0);
}
template <int CTRL> __device__ __forceinline__ int dpp0(int x) {
  return __builtin_amdgcn_update_dpp(0, x, CTRL, 0xf, 0xf, true);
}
__device__ __forceinline__ unsigned fix(float x) { return min(__float_as_uint(x), EMPTY - 1u); }
__device__ __forceinline__ unsigned ff1(unsigned long long m) { unsigned d; asm("s_ff1_i32_b64 %0, %1" : "=s"(d) : "s"(m)); return d; }
__device__ __forceinline__ unsigned pkmin(unsigned a, unsigned b) {
  return __builtin_bit_cast(unsigned, __builtin_elementwise_min(__builtin_bit_cast(u16x2, a), __builtin_bit_cast(u16x2, b)));
}
#define PIN_S(x) asm volatile("" : "+s"(x))
#define BARRIER() asm volatile("s_waitcnt lgkmcnt(0)\n\ts_barrier" ::: "memory")

__global__ void __launch_bounds__(NTHR, 2)
attn_radix(const unsigned char* __restrict__ q, const unsigned char* __restrict__ kc, const unsigned char* __restrict__ vc,
           const int* __restrict__ bt, const int* __restrict__ ctx, unsigned char* __restrict__ out,
           unsigned char* __restrict__ ws, unsigned long long kv_bytes) {
  __shared__ __attribute__((aligned(16))) unsigned char lds[L_END];
  const int tid = threadIdx.x, lane = tid & 63, w = __builtin_amdgcn_readfirstlane(tid >> 6);
  const int g = lane >> 4, r = lane & 15;
  STAMP(0); PSTAMP(0); QSTAMP(0); STAMP2(3, 0); STAMP2(4, 0);
  asm volatile("" :: "s"(q), "s"(kc), "s"(vc), "s"(bt), "s"(ctx), "s"(out), "s"(ws), "s"(kv_bytes));
  const int craw = ctx[lane & 7];
  const i32x4 bz = *reinterpret_cast<const i32x4*>(reinterpret_cast<const unsigned char*>(bt) + lane * 16);
  const i32x4 ba = *reinterpret_cast<const i32x4*>(reinterpret_cast<const unsigned char*>(bt) + tid * 16);
  __builtin_amdgcn_sched_barrier(0);
  PSTAMP(1);
  const int ak = (int)((size_t)kc >> 8) & 3, av = (int)((size_t)vc >> 8) & 3;
  const bool skew = ak == av;
  const int slow = (1 - ak) & 3, nsl = skew ? NSL : 32, nf = skew ? NF : 32;
  int kvh, wg, nch;
  {
    const int id = SPREAD ? blockIdx.x : (blockIdx.x & 7) * 32 + (blockIdx.x >> 3), rem = id & 127, t = rem - nsl;
    const int qd = (t * (skew ? (65536 + NF - 1) / NF : 2048)) >> 16;
    kvh = (slow + (id >> 7) * 4 + (t < 0 ? 0 : 1 + qd)) & 7;
    wg = t < 0 ? (REV ? -1 - t : rem) : t - qd * nf;
    nch = t < 0 ? nsl : nf;
  }
  const u32x4 qv = *reinterpret_cast<const u32x4*>(q + (w * H + kvh * 4) * D * 2 + lane * 16);
  const int c0 = ctx[r >> 2], c1 = ctx[4 + (r >> 2)];
  const int ckvh = (slow + 4 + (blockIdx.x & 7)) & 7, crow = blockIdx.x >> 3;
  const int cnch = (blockIdx.x & 3) == 0 ? nsl : nf;
  const __amdgpu_buffer_rsrc_t rsK = __builtin_amdgcn_make_buffer_rsrc((void*)kc, 0, (unsigned)kv_bytes, 0x00020000);
  const __amdgpu_buffer_rsrc_t rsV = __builtin_amdgcn_make_buffer_rsrc((void*)vc, 0, (unsigned)kv_bytes, 0x00020000);
  const __amdgpu_buffer_rsrc_t rsW = __builtin_amdgcn_make_buffer_rsrc((void*)ws, 0, W_END, 0x00020000);
  const int mycell = W_CELL + (ckvh * ROWS + crow) * 64, myrow = W_PART + (ckvh * ROWS + crow) * ROW_B;
  const int pairoff = PAY + (lane >> 5) * SLOT_B + (lane & 31) * 16;
  const u32x4 e4 = {EMPTY, EMPTY, EMPTY, EMPTY};
  u32x2 epoch = __builtin_amdgcn_raw_buffer_load_b64(rsW, mycell, 0, AUX_SC1);
  auto k32 = [&](int c, int rr) -> unsigned {
    return c == NSL ? (rr == 1 ? rcp32(8 * NSL) : rr == 2 ? rcp32(16 * NSL) : rcp32(24 * NSL))
         : c == NF ? (rr == 1 ? rcp32(8 * NF) : rr == 2 ? rcp32(16 * NF) : rcp32(24 * NF)) : (rr == 1 ? rcp32(256) : rr == 2 ? rcp32(512) : rcp32(768));
  };
  unsigned k1 = k32(nch, 1), k2 = k32(nch, 2), k3 = k32(nch, 3);
  int cap = nch * CHUNK, u1 = wg * 8 + w, khead = kvh * 256;
  const int xr = (r ^ g) * 16;
  int kofs[4], vofs = 4 * g * TOK_BYTES + r * 16;
#pragma unroll
  for (int i = 0; i < 4; ++i) kofs[i] = (4 * i + g) * TOK_BYTES + (xr ^ (64 * i));
  PIN_S(k1); PIN_S(k2); PIN_S(k3); PIN_S(cap); PIN_S(u1); PIN_S(khead);
  asm volatile("" : "+v"(kofs[0]), "+v"(kofs[1]), "+v"(kofs[2]), "+v"(kofs[3]), "+v"(vofs));
  const int nblk = (((craw <= 0 || craw > LK) ? LK : craw) + BS - 1) >> 4;
  int cum = nblk + dpp0<0x111>(nblk); cum += dpp0<0x112>(cum); cum += dpp0<0x114>(cum);
  cum += (r >= 4) ? GAP : 0;
  int mn = 256 - nblk; mn = max(mn, dpp0<0x111>(mn)); mn = max(mn, dpp0<0x112>(mn)); mn = max(mn, dpp0<0x114>(mn));
  int minblk = 256 - __builtin_amdgcn_readlane(mn, 7), total = __builtin_amdgcn_readlane(cum, 7);
  int hi_a = r < 8 ? cum : 0, hi_b = r < 8 ? -r : (r == 8), lo_a = r < 8 ? cum - nblk : 0, lo_b = r < 8 ? 1 - r : 0;
  int btrow = L_BT + (r < 8 ? r : 0) * NB * 4 - 4 * lo_a, bt_b = r < 8 ? 4 * r : 0;
  PIN_S(minblk); PIN_S(total);
  asm volatile("" : "+v"(hi_a), "+v"(hi_b), "+v"(lo_a), "+v"(lo_b), "+v"(btrow), "+v"(bt_b));
  __builtin_amdgcn_sched_barrier(0);
  {
    const unsigned n0 = 4 * ff1(__ballot(ba.x != bz.x)), n1 = 4 * ff1(__ballot(ba.y != bz.y)) + 1, n2 = 4 * ff1(__ballot(ba.z != bz.z)) + 2,
                   n3 = 4 * ff1(__ballot(ba.w != bz.w)) + 3;
    const unsigned ns = min(min(n0, n1), min(n2, n3));
    PSTAMP(2);
    *reinterpret_cast<unsigned short*>(lds + L_NS + 2 * w) = (unsigned short)ns;
    *reinterpret_cast<i32x4*>(lds + L_BT + tid * 16) = ba;
    if (tid == 0) *reinterpret_cast<u32x2*>(lds + L_CNT) = u32x2{0u, 0u};
  }
  if (w == 0) {
#pragma unroll
    for (int k = 0; k < NEARLY; k += 2) __builtin_amdgcn_raw_buffer_store_b128(e4, rsW, pairoff, myrow + k * SLOT_B, AUX_SC1);
    __builtin_amdgcn_raw_buffer_store_b128(e4, rsW, lane * 16, myrow, AUX_SC1);
  }
  BARRIER();
  STAMP(1); PSTAMP(3);
  int NS;
  {
    const u32x4 n = *reinterpret_cast<const u32x4*>(lds + L_NS);
    const unsigned m = pkmin(pkmin(n.x, n.y), pkmin(n.z, n.w));
    NS = min(__builtin_amdgcn_readfirstlane(min(m & 0xffffu, m >> 16)), minblk);
  }
  const int pad = (NS * WNUM) >> 3, UW = total - 7 * NS + pad;
  const int seg_hi = __mul24(hi_b, NS) + hi_a, seg_lo = __mul24(lo_b, NS) + lo_a, btadr = __mul24(bt_b, NS) + btrow;
  auto edge = [&](unsigned t, unsigned k) { const int x = (int)__umulhi(t, k); return max(x - pad, min((x * WMUL) >> 16, NS)); };
  auto producers = [&](int b, int lim, unsigned k) {
    const int lo = __builtin_amdgcn_readlane(seg_lo, b), hi = __builtin_amdgcn_readlane(seg_hi, b);
    const int a = edge(lane * 8 * UW, k), e = edge((lane + 1) * 8 * UW, k);
    int cnt = __builtin_popcountll(__ballot(lane < lim && a < e && (a < NS || (a < hi && e > lo))));
    if (lim > 64) {
      const int a2 = edge((lane + 64) * 8 * UW, k), e2 = edge((lane + 65) * 8 * UW, k);
      cnt += __builtin_popcountll(__ballot(lane + 64 < lim && a2 < e2 && (a2 < NS || (a2 < hi && e2 > lo))));
    }
    return cnt;
  };
  const int nrounds = UW < cap ? 1 : UW < 2 * cap ? 2 : 3;
  const unsigned kk = UW < cap ? k1 : UW < 2 * cap ? k2 : k3;
  int np = 0;
  constexpr float C2 = 0.088388347648318440f * 1.4426950408889634f;

  auto chunk = [&](const int round) __attribute__((always_inline)) {
    const int v = wg * nrounds + round;
    const unsigned t0 = (unsigned)((round == 0 && UW < cap) ? u1 : v * 8 + w) * UW;
    const int ws0 = edge(t0, kk), ws1 = edge(t0 + UW, kk);
    const int f = ws0 + g;
    const bool in = (f >= seg_lo) & (f < min(seg_hi, ws1));
    const int ph = *reinterpret_cast<const int*>(lds + (in ? btadr + 4 * f : L_BT));
    const unsigned long long im = __ballot(in);
    const int sov = (ph << 15) + khead;
    int so[BPW];
    if (round == 0 && (w == 0 || w >= WAVES - NPOLL)) {
      const int ccap = cnch * CHUNK, cr = UW < ccap ? 1 : UW < 2 * ccap ? 2 : 3;
      np = producers(crow >> 2, cnch * cr, k32(cnch, cr));
      const unsigned long long e1 = (((unsigned long long)epoch.y << 32) | epoch.x) + 1ull;
      epoch = u32x2{(unsigned)e1, (unsigned)(e1 >> 32)};
      if (w == 0) {
        for (int k = NEARLY; k < np; k += 2)
          if (k + (lane >> 5) < np) __builtin_amdgcn_raw_buffer_store_b128(e4, rsW, pairoff, myrow + k * SLOT_B, AUX_SC1);
        asm volatile("s_waitcnt vmcnt(0)" ::: "memory");
        if (lane == 0) __builtin_amdgcn_raw_buffer_store_b64(epoch, rsW, mycell + 8, 0, AUX_SC1);
      }
    }
    PSTAMP(4);
#pragma unroll
    for (int t = 0; t < BPW; ++t) {
      const unsigned long long mt = im & (0xffffull << (16 * t));
      const int sx = __builtin_amdgcn_readlane(sov, ff1(mt) & 63);
      so[t] = mt ? sx : OOB;
#pragma unroll
      for (int i = 0; i < 4; ++i)
        __builtin_amdgcn_raw_ptr_buffer_load_lds(rsK, (LAS void*)(lds + L_O + w * WREG + t * 4096 + i * 1024), 16, kofs[i], so[t], 0, K_AUX);
    }
    PSTAMP(5);
    int jb[BPW], mk[BPW];
    {
      int pk = in ? ((r == 8) ? (f | 0xff0000) : ((f - seg_lo + NS) | (0x10000 << (r & 7)))) : 0;
      pk |= dpp0<0x111>(pk); pk |= dpp0<0x112>(pk); pk |= dpp0<0x114>(pk); pk |= dpp0<0x118>(pk);
#pragma unroll
      for (int t = 0; t < BPW; ++t) { const int p = __builtin_amdgcn_readlane(pk, 16 * t + 15); jb[t] = p & 0xffff; mk[t] = p >> 16; }
    }
    const int wmask = mk[0] | mk[1] | mk[2] | mk[3];
    const int cl[2] = {(c0 <= 0 || c0 > LK) ? LK : c0, (c1 <= 0 || c1 > LK) ? LK : c1};
    int lim[2][BPW];
#pragma unroll
    for (int rt = 0; rt < 2; ++rt)
#pragma unroll
      for (int t = 0; t < BPW; ++t) { lim[rt][t] = ((mk[t] >> (4 * rt + (r >> 2))) & 1) ? cl[rt] - jb[t] * BS - 4 * g : 0; asm volatile("" : "+v"(lim[rt][t])); }
    const int s0 = edge(v * 8 * UW, kk), s1 = edge((v + 1) * 8 * UW, kk);
    const int cmask = (s1 <= s0) ? 0 : (s0 < NS) ? 0xff : (int)(__ballot(lane < 8 && s0 < seg_hi && s1 > seg_lo));
    int tb[2], tr[2];
    {
      int rem = cmask;
      for (int k = 0; k < (w >> 1); ++k) rem &= rem - 1;
#pragma unroll
      for (int it = 0; it < 2; ++it) {
        tb[it] = rem ? __builtin_ctz(rem) : -1;
        tr[it] = rem ? producers(tb[it], v, kk) : 0;
        rem &= rem - 1; rem &= rem - 1; rem &= rem - 1; rem &= rem - 1;
      }
    }
    bool plain[2];
#pragma unroll
    for (int rt = 0; rt < 2; ++rt)
      plain[rt] = FASTMASK && __all((min(min(lim[rt][0], lim[rt][1]), min(lim[rt][2], lim[rt][3])) | (((cmask >> (4 * rt + (r >> 2))) & 1) ? 0 : 4)) >= 4);
    if (round == 0) {
      const bool dead = __builtin_amdgcn_readlane(craw, w) <= 0;
      *reinterpret_cast<u32x4*>(lds + L_Q + (w * 4 + g) * QROW + r * 16) = dead ? u32x4{0u, 0u, 0u, 0u} : qv;
    }
    __builtin_amdgcn_sched_barrier(0);
    BARRIER();
    u32x4 cell = {0u, 0u, 1u, 0u};
    if (w == 0) cell = __builtin_amdgcn_raw_buffer_load_b128(rsW, W_CELL + (kvh * ROWS + (lane & 31)) * 64, 0, AUX_SC1);
    __builtin_amdgcn_sched_barrier(0);
    u32x4 vr[BPW][4];
#pragma unroll
    for (int t = 0; t < BPW; ++t)
#pragma unroll
      for (int i = 0; i < 4; ++i) vr[t][i] = __builtin_amdgcn_raw_buffer_load_b128(rsV, vofs + i * TOK_BYTES, so[t], V_AUX);
    __builtin_amdgcn_sched_barrier(0);
    STAMP(2); PSTAMP(6); QSTAMP(1);
    asm volatile("s_waitcnt vmcnt(16)" ::: "memory");
    QSTAMP(2);
    u32x4 pf[2][2];
#pragma unroll
    for (int rt = 0; rt < 2; ++rt) {
      float m = -INFINITY, l = 0.f;
      pf[rt][0] = u32x4{0, 0, 0, 0}; pf[rt][1] = u32x4{0, 0, 0, 0};
      if ((wmask >> (4 * rt)) & 0xf) {
        u32x4 qf[4], kf[BPW][4];
        f32x4 s[BPW];
#pragma unroll
        for (int i = 0; i < 4; ++i) {
          qf[i] = *reinterpret_cast<const u32x4*>(lds + L_Q + (16 * rt + r) * QROW + 64 * i + 16 * g);
#pragma unroll
          for (int t = 0; t < BPW; ++t) kf[t][i] = *reinterpret_cast<const u32x4*>(lds + L_O + w * WREG + t * 4096 + r * 256 + (xr ^ (64 * i)));
        }
#pragma unroll
        for (int t = 0; t < BPW; ++t) s[t] = f32x4{0.f, 0.f, 0.f, 0.f};
#pragma unroll
        for (int i = 0; i < 4; ++i)
#pragma unroll
          for (int t = 0; t < BPW; ++t) s[t] = mfma(kf[t][i], qf[i], s[t]);
        if (!plain[rt]) {
#pragma unroll
          for (int t = 0; t < BPW; ++t)
#pragma unroll
            for (int e = 0; e < 4; ++e) s[t][e] = (e < lim[rt][t]) ? s[t][e] : -INFINITY;
        }
#pragma unroll
        for (int t = 0; t < BPW; ++t) m = fmaxf(fmaxf(fmaxf(fmaxf(m, s[t][0]), s[t][1]), s[t][2]), s[t][3]);
        m = xg_max(m) * C2;
        const float ms = (m == -INFINITY) ? 0.f : -m;
#pragma unroll
        for (int t = 0; t < BPW; ++t) {
          f32x4 p;
#pragma unroll
          for (int e = 0; e < 4; ++e) p[e] = __builtin_amdgcn_exp2f(fmaf(s[t][e], C2, ms));
          pf[rt][t >> 1][2 * (t & 1)] = pack_bf16(p[0], p[1]);
          pf[rt][t >> 1][2 * (t & 1) + 1] = pack_bf16(p[2], p[3]);
        }
        const u32x4 ones = {0x3f803f80u, 0x3f803f80u, 0x3f803f80u, 0x3f803f80u};
        l = mfma(ones, pf[rt][1], mfma(ones, pf[rt][0], f32x4{0.f, 0.f, 0.f, 0.f}))[0];
      }
      if (g == 0) {
        *reinterpret_cast<float*>(lds + L_MX + ((16 * rt + r) * WAVES + w) * 4) = m;
        *reinterpret_cast<float*>(lds + L_LS + ((16 * rt + r) * WAVES + w) * 4) = l;
      }
    }
    asm volatile("s_waitcnt lgkmcnt(0)" ::: "memory");
    if (lane == 0) __hip_atomic_fetch_add(reinterpret_cast<int*>(lds + L_CNT), 1, __ATOMIC_RELAXED, __HIP_MEMORY_SCOPE_WORKGROUP);
    STAMP(3); QSTAMP(3);
    f32x4 acc[2][8];
#pragma unroll
    for (int j = 0; j < 8; ++j) { acc[0][j] = f32x4{0.f, 0.f, 0.f, 0.f}; acc[1][j] = f32x4{0.f, 0.f, 0.f, 0.f}; }
#pragma unroll
    for (int pr = 0; pr < 2; ++pr) {
      u32x4 vt[8];
#pragma unroll
      for (int c = 0; c < 4; ++c) {
#pragma unroll
        for (int j = 0; j < 8; ++j)
          vt[j][c] = __builtin_amdgcn_perm(vr[2 * pr + (c >> 1)][2 * (c & 1) + 1][j >> 1], vr[2 * pr + (c >> 1)][2 * (c & 1)][j >> 1], (j & 1) ? 0x07060302u : 0x05040100u);
        __builtin_amdgcn_sched_barrier(0);
      }
      if (wmask & 0x0f) {
#pragma unroll
        for (int j = 0; j < 8; ++j) acc[0][j] = mfma(vt[j], pf[0][pr], acc[0][j]);
      }
      if (wmask & 0xf0) {
#pragma unroll
        for (int j = 0; j < 8; ++j) acc[1][j] = mfma(vt[j], pf[1][pr], acc[1][j]);
      }
      if (pr == 0) QSTAMP(4);
    }
    STAMP(4); QSTAMP(5);
#pragma unroll
    for (int rt = 0; rt < 2; ++rt) {
      if ((cmask >> (4 * rt + (r >> 2))) & 1) {
#pragma unroll
        for (int j = 0; j < 8; ++j) *reinterpret_cast<f32x4*>(lds + L_O + w * WREG + (16 * rt + r) * OROW + (4 * j + g) * 16) = acc[rt][j];
      }
    }
    if (w == 0) {
      const bool need = (cmask >> ((lane & 31) >> 2)) & 1;
#pragma unroll 1
      for (unsigned spins = 0; spins < SPIN_CAP; ++spins) {
        const unsigned long long e1 = (((unsigned long long)cell.y << 32) | cell.x) + 1ull;
        const bool ok = !need || (cell.z == (unsigned)e1 && cell.w == (unsigned)(e1 >> 32));
        if (__all(ok)) break;
        __builtin_amdgcn_s_sleep(2);
        asm volatile("" ::: "memory");
        cell = __builtin_amdgcn_raw_buffer_load_b128(rsW, W_CELL + (kvh * ROWS + (lane & 31)) * 64, 0, AUX_SC1);
      }
    }
    float wt[2][WAVES], Mr[2], Lr[2];
    int oadr[2], gofs[2];
    if (tb[0] >= 0) {
      while (__hip_atomic_load(reinterpret_cast<int*>(lds + L_CNT), __ATOMIC_RELAXED, __HIP_MEMORY_SCOPE_WORKGROUP) < WAVES * (round + 1)) __builtin_amdgcn_s_sleep(1);
      asm volatile("" ::: "memory");
    }
#pragma unroll
    for (int it = 0; it < 2; ++it) {
      if (tb[it] >= 0) {
        const int row = 4 * tb[it] + ((tid & 127) >> 5);
        const f32x4 m0 = *reinterpret_cast<const f32x4*>(lds + L_MX + row * WAVES * 4), m1 = *reinterpret_cast<const f32x4*>(lds + L_MX + row * WAVES * 4 + 16);
        const f32x4 l0 = *reinterpret_cast<const f32x4*>(lds + L_LS + row * WAVES * 4), l1 = *reinterpret_cast<const f32x4*>(lds + L_LS + row * WAVES * 4 + 16);
        const float M = fmax3(fmax3(m0.x, m0.y, m0.z), fmax3(m0.w, m1.x, m1.y), fmax3(m1.z, m1.w, m1.w)), Ms = (M == -INFINITY) ? 0.f : M;
        float L = 0.f;
#pragma unroll
        for (int ww = 0; ww < WAVES; ++ww) {
          wt[it][ww] = __builtin_amdgcn_exp2f((ww < 4 ? m0[ww & 3] : m1[ww & 3]) - Ms);
          L = fmaf(wt[it][ww], ww < 4 ? l0[ww & 3] : l1[ww & 3], L);
          asm volatile("" : "+v"(wt[it][ww]));
        }
        Mr[it] = __uint_as_float(fix(M)); Lr[it] = __uint_as_float(fix(L));
        oadr[it] = L_O + row * OROW + 16 * (tid & 31);
        gofs[it] = W_PART + (kvh * ROWS + row) * ROW_B;
        asm volatile("" : "+v"(Mr[it]), "+v"(Lr[it]), "+v"(oadr[it]), "+v"(gofs[it]));
      }
    }
    __builtin_amdgcn_s_waitcnt(0x0F70);
    QSTAMP(6);
    BARRIER();
    STAMP(5);
#pragma unroll
    for (int it = 0; it < 2; ++it) {
      if (tb[it] >= 0) {
        f32x4 x[WAVES];
#pragma unroll
        for (int ww = 0; ww < WAVES; ++ww) x[ww] = *reinterpret_cast<const f32x4*>(lds + oadr[it] + ww * WREG);
        if ((tid & 31) == 0) __builtin_amdgcn_raw_buffer_store_b64(u32x2{__float_as_uint(Mr[it]), __float_as_uint(Lr[it])}, rsW, gofs[it], tr[it] * 8, AUX_SC1);
        f32x4 o = x[0] * wt[it][0];
#pragma unroll
        for (int ww = 1; ww < WAVES; ++ww) o += x[ww] * wt[it][ww];
        __builtin_amdgcn_raw_buffer_store_b128(u32x4{fix(o.x), fix(o.y), fix(o.z), fix(o.w)}, rsW, gofs[it] + 16 * (tid & 31), PAY + tr[it] * SLOT_B, AUX_SC1);
      }
    }
  };
  chunk(0);
#pragma unroll 1
  for (int round = 1; round < nrounds; ++round) { BARRIER(); chunk(round); }
  STAMP(6);
  if (w >= WAVES - NPOLL) {
    const int p4 = lane & 31, hf = lane >> 5;
    float M = -INFINITY, L = 0.f;
    f32x4 o = {0.f, 0.f, 0.f, 0.f};
    struct Set { u32x4 x[PG]; u32x2 ml[PG]; };
    auto fetch = [&](Set& s, int k0) __attribute__((always_inline)) {
      asm volatile("" ::: "memory");
#pragma unroll
      for (int i = 0; i < PG; ++i) {
        const int k = k0 + 2 * i + hf, dead = (np - 1 - k) & OOB;
        s.x[i] = __builtin_amdgcn_raw_buffer_load_b128(rsW, (PAY + k * SLOT_B + 16 * p4) | dead, myrow, AUX_SC1);
        s.ml[i] = __builtin_amdgcn_raw_buffer_load_b64(rsW, (k * 8) | dead, myrow, AUX_SC1);
      }
      asm volatile("" ::: "memory");
    };
    auto complete = [&](const Set& s) __attribute__((always_inline)) {
      unsigned top = 0;
#pragma unroll
      for (int i = 0; i < PG; ++i) top = max(max(max(top, s.x[i].x), max(s.x[i].y, s.x[i].z)), max(max(s.x[i].w, s.ml[i].x), s.ml[i].y));
      return __all(top != EMPTY);
    };
    auto absorb = [&](const Set& s, int k0) __attribute__((always_inline)) {
      float mg = M;
#pragma unroll
      for (int i = 0; i < PG; ++i) mg = fmaxf(mg, (k0 + 2 * i + hf >= np) ? -INFINITY : __uint_as_float(s.ml[i].x));
      mg = xh_max(mg);
      const float ms = (mg == -INFINITY) ? 0.f : mg;
      const float al = __builtin_amdgcn_exp2f(M - ms);
      o *= al; L *= al; M = mg;
#pragma unroll
      for (int i = 0; i < PG; ++i) {
        const float wv = (k0 + 2 * i + hf >= np) ? 0.f : __builtin_amdgcn_exp2f(__uint_as_float(s.ml[i].x) - ms);
        o += __builtin_bit_cast(f32x4, s.x[i]) * wv;
        L = fmaf(wv, __uint_as_float(s.ml[i].y), L);
      }
    };
    auto finish = [&]() __attribute__((always_inline)) {
#pragma unroll
      for (int e = 0; e < 4; ++e) o[e] = xh_sum(o[e]);
      const float inv = __builtin_amdgcn_rcpf(xh_sum(L));
      const unsigned pk = pack_bf16((hf ? o[2] : o[0]) * inv, (hf ? o[3] : o[1]) * inv);
      const __amdgpu_buffer_rsrc_t rsO = __builtin_amdgcn_make_buffer_rsrc((void*)out, 0, B * H * D * 2, 0x00020000);
      const int ob = ((crow >> 2) * H + ckvh * 4 + (crow & 3)) * D * 2, ov = (32 * (p4 & 3) + 16 * hf + (p4 >> 2)) * 2;
      __builtin_amdgcn_raw_buffer_store_b16((unsigned short)pk, rsO, ov, ob, OUT_AUX);
      __builtin_amdgcn_raw_buffer_store_b16((unsigned short)(pk >> 16), rsO, ov + 16, ob, OUT_AUX);
      if (lane == 0) __builtin_amdgcn_raw_buffer_store_b64(epoch, rsW, mycell, 0, OUT_AUX);
    };
    STAMP(1);
    if (NPOLL > 1 && np <= 2 * PG) {
      for (int k = WAVES - 1 - w; k > 0; --k) __builtin_amdgcn_s_sleep(DPOLL);
      bool quit = false;
#pragma unroll 1
      for (unsigned spins = 0; !quit; ++spins) {
        Set sa;
        fetch(sa, 0);
#pragma unroll 1
        for (int t = 0; t < 1024; ++t) {
          quit = __hip_atomic_load(reinterpret_cast<int*>(lds + L_CNT + 4), __ATOMIC_RELAXED, __HIP_MEMORY_SCOPE_WORKGROUP) != 0;
          if (quit || (__builtin_amdgcn_s_getreg(7 | (31 << 11)) & 0xc000000fu) == 0) break;
        }
        if (quit) break;
        if (complete(sa) || spins >= SPIN_CAP) {
          __hip_atomic_store(reinterpret_cast<int*>(lds + L_CNT + 4), 1, __ATOMIC_RELAXED, __HIP_MEMORY_SCOPE_WORKGROUP);
          absorb(sa, 0); finish();
          quit = true;
        }
      }
    } else if (w == WAVES - 1) {
      for (int k0 = 0; k0 < np; k0 += 2 * PG) {
        Set sa;
        fetch(sa, k0);
#pragma unroll 1
        for (unsigned spins = 0; spins < SPIN_CAP && !complete(sa); ++spins) { __builtin_amdgcn_s_sleep(1); fetch(sa, k0); }
        absorb(sa, k0);
      }
      finish();
    }
    STAMP(2);
  }
}
}

extern "C" void kernel_launch(void* const* d_in, const int* in_sizes, int n_in, void* d_out, int out_size, void* d_ws,
                              size_t ws_size, hipStream_t stream) {
  attn_radix<<<dim3(HKV * ROWS), dim3(NTHR), 0, stream>>>((const unsigned char*)d_in[0], (const unsigned char*)d_in[1],
                                                           (const unsigned char*)d_in[2], (const int*)d_in[3],
                                                           (const int*)d_in[4], (unsigned char*)d_out, (unsigned char*)d_ws,
                                                           (unsigned long long)in_sizes[1] * 2ull);
}
```

```cpp
#include <hip/hip_runtime.h>
#include <hip/hip_bf16.h>
#include <stdint.h>

typedef __attribute__((ext_vector_type(8))) __bf16 bf16x8;
typedef __attribute__((ext_vector_type(4))) float f32x4;
typedef __attribute__((ext_vector_type(4))) unsigned u32x4;
typedef __attribute__((ext_vector_type(2))) unsigned u32x2;
typedef __attribute__((ext_vector_type(4))) int i32x4;
typedef __attribute__((ext_vector_type(2))) unsigned short u16x2;
#define LAS __attribute__((address_space(3)))

#define K_AUX 2
#define V_AUX 2
#define WNUM 3
#define NSL 38
#define HOIST 0
#define FASTMASK 1
#define OUT_AUX 0
#define SPREAD 0
#define GAP 3
#define REV 1
#define PG 7
#define CODEPF 32
#define NEARLY 14
#define STAMP(i)
#define PSTAMP(i)
#define QSTAMP(i)
#define STAMP2(s, i)

namespace {
constexpr int B = 8, H = 32, HKV = 8, D = 128, BS = 16, NB = 256, LK = NB * BS;
constexpr int NF = (128 - NSL) / 3;
static_assert(NSL + 3 * NF == 128, "four heads share 128 workgroups");
constexpr int WAVES = 8, NTHR = 512;
constexpr int BPW = 4;
constexpr int CHUNK = WAVES * BPW;
constexpr int ROWS = 32;
constexpr int TOK_BYTES = HKV * D * 2;
constexpr int QROW = 272;
constexpr int OROW = 528;
constexpr int WREG = ROWS * OROW;
constexpr int AUX_SC1 = 16;
constexpr int WMUL = (8 * 65536 + 8 + WNUM - 1) / (8 + WNUM);
constexpr int L_O = 0;
constexpr int L_BT = L_O + WAVES * WREG;
constexpr int L_Q = L_BT + B * NB * 4;
constexpr int L_MX = L_Q + ROWS * QROW;
constexpr int L_LS = L_MX + ROWS * WAVES * 4;
constexpr int L_NS = L_LS + ROWS * WAVES * 4;
constexpr int L_CNT = L_NS + 16;
constexpr int L_END = L_CNT + 16;
constexpr int NSLOT = 96, ML_B = 1024, SLOT_B = 1024, PAY = ML_B + 512, ROW_B = ML_B + NSLOT * SLOT_B;
constexpr int W_CELL = 0, W_PART = HKV * ROWS * 64, W_END = W_PART + HKV * ROWS * ROW_B;
constexpr unsigned EMPTY = 0xffffffffu;
constexpr unsigned SPIN_CAP = 1u << 22;
constexpr int OOB = (int)0x80000000;
constexpr unsigned rcp32(unsigned n) { return (unsigned)(((1ull << 32) + n - 1) / n); }

__device__ __forceinline__ float fmax3(float a, float b, float c) { float d; asm("v_max3_f32 %0, %1, %2, %3" : "=v"(d) : "v"(a), "v"(b), "v"(c)); return d; }
__device__ __forceinline__ float fmax2(float a, float b) { float d; asm("v_max_f32 %0, %1, %2" : "=v"(d) : "v"(a), "v"(b)); return d; }
__device__ __forceinline__ float xg_max(float x) {
  auto s = __builtin_amdgcn_permlane16_swap(__float_as_uint(x), __float_as_uint(x), false, false);
  x = fmax2(__uint_as_float(s[0]), __uint_as_float(s[1]));
  auto t = __builtin_amdgcn_permlane32_swap(__float_as_uint(x), __float_as_uint(x), false, false);
  return fmax2(__uint_as_float(t[0]), __uint_as_float(t[1]));
}
__device__ __forceinline__ float xg_sum(float x) {
  auto s = __builtin_amdgcn_permlane16_swap(__float_as_uint(x), __float_as_uint(x), false, false);
  x = __uint_as_float(s[0]) + __uint_as_float(s[1]);
  auto t = __builtin_amdgcn_permlane32_swap(__float_as_uint(x), __float_as_uint(x), false, false);
  return __uint_as_float(t[0]) + __uint_as_float(t[1]);
}
__device__ __forceinline__ float xh_max(float x) {
  auto t = __builtin_amdgcn_permlane32_swap(__float_as_uint(x), __float_as_uint(x), false, false);
  return fmaxf(__uint_as_float(t[0]), __uint_as_float(t[1]));
}
__device__ __forceinline__ float xh_sum(float x) {
  auto t = __builtin_amdgcn_permlane32_swap(__float_as_uint(x), __float_as_uint(x), false, false);
  return __uint_as_float(t[0]) + __uint_as_float(t[1]);
}
__device__ __forceinline__ unsigned pack_bf16(float lo, float hi) {
  typedef __attribute__((ext_vector_type(2))) __bf16 bf16x2;
  bf16x2 v = {(__bf16)lo, (__bf16)hi};
  return __builtin_bit_cast(unsigned, v);
}
__device__ __forceinline__ f32x4 mfma(u32x4 a, u32x4 b, f32x4 c) {
  return __builtin_amdgcn_mfma_f32_16x16x32_bf16(__builtin_bit_cast(bf16x8, a), __builtin_bit_cast(bf16x8, b), c, 0, 0, 0);
}
template <int CTRL> __device__ __forceinline__ int dpp0(int x) {
  return __builtin_amdgcn_update_dpp(0, x, CTRL, 0xf, 0xf, true);
}
__device__ __forceinline__ unsigned fix(float x) { return min(__float_as_uint(x), EMPTY - 1u); }
__device__ __forceinline__ unsigned ff1(unsigned long long m) { unsigned d; asm("s_ff1_i32_b64 %0, %1" : "=s"(d) : "s"(m)); return d; }
__device__ __forceinline__ unsigned pkmin(unsigned a, unsigned b) {
  return __builtin_bit_cast(unsigned, __builtin_elementwise_min(__builtin_bit_cast(u16x2, a), __builtin_bit_cast(u16x2, b)));
}
#define PIN_S(x) asm volatile("" : "+s"(x))
#define BARRIER() asm volatile("s_waitcnt lgkmcnt(0)\n\ts_barrier" ::: "memory")

__global__ void __launch_bounds__(NTHR, 2)
attn_radix(const unsigned char* __restrict__ q, const unsigned char* __restrict__ kc, const unsigned char* __restrict__ vc,
           const int* __restrict__ bt, const int* __restrict__ ctx, unsigned char* __restrict__ out,
           unsigned char* __restrict__ ws, unsigned long long kv_bytes) {
  __shared__ __attribute__((aligned(16))) unsigned char lds[L_END];
  const int tid = threadIdx.x, lane = tid & 63, w = __builtin_amdgcn_readfirstlane(tid >> 6);
  const int g = lane >> 4, r = lane & 15;
  STAMP(0); PSTAMP(0); QSTAMP(0); STAMP2(3, 0); STAMP2(4, 0);
  asm volatile("" :: "s"(q), "s"(kc), "s"(vc), "s"(bt), "s"(ctx), "s"(out), "s"(ws), "s"(kv_bytes));
  const int craw = ctx[lane & 7];
  const i32x4 bz = *reinterpret_cast<const i32x4*>(reinterpret_cast<const unsigned char*>(bt) + lane * 16);
  const i32x4 ba = *reinterpret_cast<const i32x4*>(reinterpret_cast<const unsigned char*>(bt) + tid * 16);
  __builtin_amdgcn_sched_barrier(0);
  PSTAMP(1);
  {
    unsigned lo, hi, len;
    asm volatile("s_getpc_b64 s[6:7]\n.Lradix_pc:\n\ts_and_b32 %0, s6, 0xffffff00\n\ts_mov_b32 %1, s7\n\ts_mov_b32 %2, .Lradix_end-.Lradix_pc"
                 : "=s"(lo), "=s"(hi), "=s"(len) :: "s6", "s7", "scc");
    const __amdgpu_buffer_rsrc_t rsC = __builtin_amdgcn_make_buffer_rsrc((void*)(((unsigned long long)hi << 32) | lo), 0, (blockIdx.x >> 3) < CODEPF ? len : 0u, 0x00020000);
    __builtin_amdgcn_raw_ptr_buffer_load_lds(rsC, (LAS void*)(lds + L_O + w * WREG + 16384), 4, tid * 128, 0, 0, 0);
  }
  const int ak = (int)((size_t)kc >> 8) & 3, av = (int)((size_t)vc >> 8) & 3;
  const bool skew = ak == av;
  const int slow = (1 - ak) & 3, nsl = skew ? NSL : 32, nf = skew ? NF : 32;
  int kvh, wg, nch;
  {
    const int id = SPREAD ? blockIdx.x : (blockIdx.x & 7) * 32 + (blockIdx.x >> 3), rem = id & 127, t = rem - nsl;
    const int qd = (t * (skew ? (65536 + NF - 1) / NF : 2048)) >> 16;
    kvh = (slow + (id >> 7) * 4 + (t < 0 ? 0 : 1 + qd)) & 7;
    wg = t < 0 ? (REV ? -1 - t : rem) : t - qd * nf;
    nch = t < 0 ? nsl : nf;
  }
  const u32x4 qv = *reinterpret_cast<const u32x4*>(q + (w * H + kvh * 4) * D * 2 + lane * 16);
  const int c0 = ctx[r >> 2], c1 = ctx[4 + (r >> 2)];
  const int ckvh = (slow + 4 + (blockIdx.x & 7)) & 7, crow = blockIdx.x >> 3;
  const int cnch = (blockIdx.x & 3) == 0 ? nsl : nf;
  const __amdgpu_buffer_rsrc_t rsK = __builtin_amdgcn_make_buffer_rsrc((void*)kc, 0, (unsigned)kv_bytes, 0x00020000);
  const __amdgpu_buffer_rsrc_t rsV = __builtin_amdgcn_make_buffer_rsrc((void*)vc, 0, (unsigned)kv_bytes, 0x00020000);
  const __amdgpu_buffer_rsrc_t rsW = __builtin_amdgcn_make_buffer_rsrc((void*)ws, 0, W_END, 0x00020000);
  const int mycell = W_CELL + (ckvh * ROWS + crow) * 64, myrow = W_PART + (ckvh * ROWS + crow) * ROW_B;
  const int pairoff = PAY + (lane >> 5) * SLOT_B + (lane & 31) * 16;
  const u32x4 e4 = {EMPTY, EMPTY, EMPTY, EMPTY};
  u32x2 epoch = __builtin_amdgcn_raw_buffer_load_b64(rsW, mycell, 0, AUX_SC1);
  auto k32 = [&](int c, int rr) -> unsigned {
    return c == NSL ? (rr == 1 ? rcp32(8 * NSL) : rr == 2 ? rcp32(16 * NSL) : rcp32(24 * NSL))
         : c == NF ? (rr == 1 ? rcp32(8 * NF) : rr == 2 ? rcp32(16 * NF) : rcp32(24 * NF)) : (rr == 1 ? rcp32(256) : rr == 2 ? rcp32(512) : rcp32(768));
  };
  unsigned k1 = k32(nch, 1), k2 = k32(nch, 2), k3 = k32(nch, 3);
  int cap = nch * CHUNK, u1 = wg * 8 + w, khead = kvh * 256;
  const int xr = (r ^ g) * 16;
  int kofs[4], vofs = 4 * g * TOK_BYTES + r * 16;
#pragma unroll
  for (int i = 0; i < 4; ++i) kofs[i] = (4 * i + g) * TOK_BYTES + (xr ^ (64 * i));
  PIN_S(k1); PIN_S(k2); PIN_S(k3); PIN_S(cap); PIN_S(u1); PIN_S(khead);
  asm volatile("" : "+v"(kofs[0]), "+v"(kofs[1]), "+v"(kofs[2]), "+v"(kofs[3]), "+v"(vofs));
  const int nblk = (((craw <= 0 || craw > LK) ? LK : craw) + BS - 1) >> 4;
  int cum = nblk + dpp0<0x111>(nblk); cum += dpp0<0x112>(cum); cum += dpp0<0x114>(cum);
  cum += (r >= 4) ? GAP : 0;
  int mn = 256 - nblk; mn = max(mn, dpp0<0x111>(mn)); mn = max(mn, dpp0<0x112>(mn)); mn = max(mn, dpp0<0x114>(mn));
  int minblk = 256 - __builtin_amdgcn_readlane(mn, 7), total = __builtin_amdgcn_readlane(cum, 7);
  int hi_a = r < 8 ? cum : 0, hi_b = r < 8 ? -r : (r == 8), lo_a = r < 8 ? cum - nblk : 0, lo_b = r < 8 ? 1 - r : 0;
  int btrow = L_BT + (r < 8 ? r : 0) * NB * 4 - 4 * lo_a, bt_b = r < 8 ? 4 * r : 0;
  PIN_S(minblk); PIN_S(total);
  asm volatile("" : "+v"(hi_a), "+v"(hi_b), "+v"(lo_a), "+v"(lo_b), "+v"(btrow), "+v"(bt_b));
  __builtin_amdgcn_sched_barrier(0);
  {
    const unsigned n0 = 4 * ff1(__ballot(ba.x != bz.x)), n1 = 4 * ff1(__ballot(ba.y != bz.y)) + 1, n2 = 4 * ff1(__ballot(ba.z != bz.z)) + 2,
                   n3 = 4 * ff1(__ballot(ba.w != bz.w)) + 3;
    const unsigned ns = min(min(n0, n1), min(n2, n3));
    PSTAMP(2);
    *reinterpret_cast<unsigned short*>(lds + L_NS + 2 * w) = (unsigned short)ns;
    *reinterpret_cast<i32x4*>(lds + L_BT + tid * 16) = ba;
    if (tid == 0) *reinterpret_cast<u32x2*>(lds + L_CNT) = u32x2{0u, 0u};
  }
  if (w == 0) {
#pragma unroll
    for (int k = 0; k < NEARLY; k += 2) __builtin_amdgcn_raw_buffer_store_b128(e4, rsW, pairoff, myrow + k * SLOT_B, AUX_SC1);
    __builtin_amdgcn_raw_buffer_store_b128(e4, rsW, lane * 16, myrow, AUX_SC1);
  }
  BARRIER();
  STAMP(1); PSTAMP(3);
  int NS;
  {
    const u32x4 n = *reinterpret_cast<const u32x4*>(lds + L_NS);
    const unsigned m = pkmin(pkmin(n.x, n.y), pkmin(n.z, n.w));
    NS = min(__builtin_amdgcn_readfirstlane(min(m & 0xffffu, m >> 16)), minblk);
  }
  const int pad = (NS * WNUM) >> 3, UW = total - 7 * NS + pad;
  const int seg_hi = __mul24(hi_b, NS) + hi_a, seg_lo = __mul24(lo_b, NS) + lo_a, btadr = __mul24(bt_b, NS) + btrow;
  auto edge = [&](unsigned t, unsigned k) { const int x = (int)__umulhi(t, k); return max(x - pad, min((x * WMUL) >> 16, NS)); };
  auto producers = [&](int b, int lim, unsigned k) {
    const int lo = __builtin_amdgcn_readlane(seg_lo, b), hi = __builtin_amdgcn_readlane(seg_hi, b);
    const int a = edge(lane * 8 * UW, k), e = edge((lane + 1) * 8 * UW, k);
    int cnt = __builtin_popcountll(__ballot(lane < lim && a < e && (a < NS || (a < hi && e > lo))));
    if (lim > 64) {
      const int a2 = edge((lane + 64) * 8 * UW, k), e2 = edge((lane + 65) * 8 * UW, k);
      cnt += __builtin_popcountll(__ballot(lane + 64 < lim && a2 < e2 && (a2 < NS || (a2 < hi && e2 > lo))));
    }
    return cnt;
  };
  const int nrounds = UW < cap ? 1 : UW < 2 * cap ? 2 : 3;
  const unsigned kk = UW < cap ? k1 : UW < 2 * cap ? k2 : k3;
  int np = 0;
  constexpr float C2 = 0.088388347648318440f * 1.4426950408889634f;

  auto chunk = [&](const int round) __attribute__((always_inline)) {
    const int v = wg * nrounds + round;
    const unsigned t0 = (unsigned)((round == 0 && UW < cap) ? u1 : v * 8 + w) * UW;
    const int ws0 = edge(t0, kk), ws1 = edge(t0 + UW, kk);
    const int f = ws0 + g;
    const bool in = (f >= seg_lo) & (f < min(seg_hi, ws1));
    const int ph = *reinterpret_cast<const int*>(lds + (in ? btadr + 4 * f : L_BT));
    const unsigned long long im = __ballot(in);
    const int sov = (ph << 15) + khead;
    int so[BPW];
    if (round == 0 && (w == 0 || w == WAVES - 1)) {
      const int ccap = cnch * CHUNK, cr = UW < ccap ? 1 : UW < 2 * ccap ? 2 : 3;
      np = producers(crow >> 2, cnch * cr, k32(cnch, cr));
      const unsigned long long e1 = (((unsigned long long)epoch.y << 32) | epoch.x) + 1ull;
      epoch = u32x2{(unsigned)e1, (unsigned)(e1 >> 32)};
      if (w == 0) {
        for (int k = NEARLY; k < np; k += 2)
          if (k + (lane >> 5) < np) __builtin_amdgcn_raw_buffer_store_b128(e4, rsW, pairoff, myrow + k * SLOT_B, AUX_SC1);
        asm volatile("s_waitcnt vmcnt(0)" ::: "memory");
        if (lane == 0) __builtin_amdgcn_raw_buffer_store_b64(epoch, rsW, mycell + 8, 0, AUX_SC1);
      }
    }
    PSTAMP(4);
#pragma unroll
    for (int t = 0; t < BPW; ++t) {
      const unsigned long long mt = im & (0xffffull << (16 * t));
      const int sx = __builtin_amdgcn_readlane(sov, ff1(mt) & 63);
      so[t] = mt ? sx : OOB;
#pragma unroll
      for (int i = 0; i < 4; ++i)
        __builtin_amdgcn_raw_ptr_buffer_load_lds(rsK, (LAS void*)(lds + L_O + w * WREG + t * 4096 + i * 1024), 16, kofs[i], so[t], 0, K_AUX);
    }
    PSTAMP(5);
    int jb[BPW], mk[BPW];
    {
      int pk = in ? ((r == 8) ? (f | 0xff0000) : ((f - seg_lo + NS) | (0x10000 << (r & 7)))) : 0;
      pk |= dpp0<0x111>(pk); pk |= dpp0<0x112>(pk); pk |= dpp0<0x114>(pk); pk |= dpp0<0x118>(pk);
#pragma unroll
      for (int t = 0; t < BPW; ++t) { const int p = __builtin_amdgcn_readlane(pk, 16 * t + 15); jb[t] = p & 0xffff; mk[t] = p >> 16; }
    }
    const int wmask = mk[0] | mk[1] | mk[2] | mk[3];
    const int cl[2] = {(c0 <= 0 || c0 > LK) ? LK : c0, (c1 <= 0 || c1 > LK) ? LK : c1};
    int lim[2][BPW];
#pragma unroll
    for (int rt = 0; rt < 2; ++rt)
#pragma unroll
      for (int t = 0; t < BPW; ++t) { lim[rt][t] = ((mk[t] >> (4 * rt + (r >> 2))) & 1) ? cl[rt] - jb[t] * BS - 4 * g : 0; asm volatile("" : "+v"(lim[rt][t])); }
    const int s0 = edge(v * 8 * UW, kk), s1 = edge((v + 1) * 8 * UW, kk);
    const int cmask = (s1 <= s0) ? 0 : (s0 < NS) ? 0xff : (int)(__ballot(lane < 8 && s0 < seg_hi && s1 > seg_lo));
    int tb[2], tr[2];
    {
      int rem = cmask;
      for (int k = 0; k < (w >> 1); ++k) rem &= rem - 1;
#pragma unroll
      for (int it = 0; it < 2; ++it) {
        tb[it] = rem ? __builtin_ctz(rem) : -1;
        tr[it] = rem ? producers(tb[it], v, kk) : 0;
        rem &= rem - 1; rem &= rem - 1; rem &= rem - 1; rem &= rem - 1;
      }
    }
    bool plain[2];
#pragma unroll
    for (int rt = 0; rt < 2; ++rt)
      plain[rt] = FASTMASK && __all((min(min(lim[rt][0], lim[rt][1]), min(lim[rt][2], lim[rt][3])) | (((cmask >> (4 * rt + (r >> 2))) & 1) ? 0 : 4)) >= 4);
    if (round == 0) {
      const bool dead = __builtin_amdgcn_readlane(craw, w) <= 0;
      *reinterpret_cast<u32x4*>(lds + L_Q + (w * 4 + g) * QROW + r * 16) = dead ? u32x4{0u, 0u, 0u, 0u} : qv;
    }
    __builtin_amdgcn_sched_barrier(0);
    BARRIER();
    u32x4 cell = {0u, 0u, 1u, 0u};
    if (w == 0) cell = __builtin_amdgcn_raw_buffer_load_b128(rsW, W_CELL + (kvh * ROWS + (lane & 31)) * 64, 0, AUX_SC1);
    __builtin_amdgcn_sched_barrier(0);
    u32x4 vr[BPW][4];
#pragma unroll
    for (int t = 0; t < BPW; ++t)
#pragma unroll
      for (int i = 0; i < 4; ++i) vr[t][i] = __builtin_amdgcn_raw_buffer_load_b128(rsV, vofs + i * TOK_BYTES, so[t], V_AUX);
    __builtin_amdgcn_sched_barrier(0);
    STAMP(2); PSTAMP(6); QSTAMP(1);
    asm volatile("s_waitcnt vmcnt(16)" ::: "memory");
    QSTAMP(2);
    u32x4 pf[2][2];
#pragma unroll
    for (int rt = 0; rt < 2; ++rt) {
      float m = -INFINITY, l = 0.f;
      pf[rt][0] = u32x4{0, 0, 0, 0}; pf[rt][1] = u32x4{0, 0, 0, 0};
      if ((wmask >> (4 * rt)) & 0xf) {
        u32x4 qf[4], kf[BPW][4];
        f32x4 s[BPW];
#pragma unroll
        for (int i = 0; i < 4; ++i) {
          qf[i] = *reinterpret_cast<const u32x4*>(lds + L_Q + (16 * rt + r) * QROW + 64 * i + 16 * g);
#pragma unroll
          for (int t = 0; t < BPW; ++t) kf[t][i] = *reinterpret_cast<const u32x4*>(lds + L_O + w * WREG + t * 4096 + r * 256 + (xr ^ (64 * i)));
        }
#pragma unroll
        for (int t = 0; t < BPW; ++t) s[t] = f32x4{0.f, 0.f, 0.f, 0.f};
#pragma unroll
        for (int i = 0; i < 4; ++i)
#pragma unroll
          for (int t = 0; t < BPW; ++t) s[t] = mfma(kf[t][i], qf[i], s[t]);
        if (!plain[rt]) {
#pragma unroll
          for (int t = 0; t < BPW; ++t)
#pragma unroll
            for (int e = 0; e < 4; ++e) s[t][e] = (e < lim[rt][t]) ? s[t][e] : -INFINITY;
        }
#pragma unroll
        for (int t = 0; t < BPW; ++t) m = fmaxf(fmaxf(fmaxf(fmaxf(m, s[t][0]), s[t][1]), s[t][2]), s[t][3]);
        m = xg_max(m) * C2;
        const float ms = (m == -INFINITY) ? 0.f : -m;
#pragma unroll
        for (int t = 0; t < BPW; ++t) {
          f32x4 p;
#pragma unroll
          for (int e = 0; e < 4; ++e) p[e] = __builtin_amdgcn_exp2f(fmaf(s[t][e], C2, ms));
          pf[rt][t >> 1][2 * (t & 1)] = pack_bf16(p[0], p[1]);
          pf[rt][t >> 1][2 * (t & 1) + 1] = pack_bf16(p[2], p[3]);
        }
        const u32x4 ones = {0x3f803f80u, 0x3f803f80u, 0x3f803f80u, 0x3f803f80u};
        l = mfma(ones, pf[rt][1], mfma(ones, pf[rt][0], f32x4{0.f, 0.f, 0.f, 0.f}))[0];
      }
      if (g == 0) {
        *reinterpret_cast<float*>(lds + L_MX + ((16 * rt + r) * WAVES + w) * 4) = m;
        *reinterpret_cast<float*>(lds + L_LS + ((16 * rt + r) * WAVES + w) * 4) = l;
      }
    }
    asm volatile("s_waitcnt lgkmcnt(0)" ::: "memory");
    if (lane == 0) __hip_atomic_fetch_add(reinterpret_cast<int*>(lds + L_CNT), 1, __ATOMIC_RELAXED, __HIP_MEMORY_SCOPE_WORKGROUP);
    STAMP(3); QSTAMP(3);
    f32x4 acc[2][8];
#pragma unroll
    for (int j = 0; j < 8; ++j) { acc[0][j] = f32x4{0.f, 0.f, 0.f, 0.f}; acc[1][j] = f32x4{0.f, 0.f, 0.f, 0.f}; }
#pragma unroll
    for (int pr = 0; pr < 2; ++pr) {
      u32x4 vt[8];
#pragma unroll
      for (int c = 0; c < 4; ++c) {
#pragma unroll
        for (int j = 0; j < 8; ++j)
          vt[j][c] = __builtin_amdgcn_perm(vr[2 * pr + (c >> 1)][2 * (c & 1) + 1][j >> 1], vr[2 * pr + (c >> 1)][2 * (c & 1)][j >> 1], (j & 1) ? 0x07060302u : 0x05040100u);
        __builtin_amdgcn_sched_barrier(0);
      }
      if (wmask & 0x0f) {
#pragma unroll
        for (int j = 0; j < 8; ++j) acc[0][j] = mfma(vt[j], pf[0][pr], acc[0][j]);
      }
      if (wmask & 0xf0) {
#pragma unroll
        for (int j = 0; j < 8; ++j) acc[1][j] = mfma(vt[j], pf[1][pr], acc[1][j]);
      }
      if (pr == 0) QSTAMP(4);
    }
    STAMP(4); QSTAMP(5);
#pragma unroll
    for (int rt = 0; rt < 2; ++rt) {
      if ((cmask >> (4 * rt + (r >> 2))) & 1) {
#pragma unroll
        for (int j = 0; j < 8; ++j) *reinterpret_cast<f32x4*>(lds + L_O + w * WREG + (16 * rt + r) * OROW + (4 * j + g) * 16) = acc[rt][j];
      }
    }
    if (w == 0) {
      const bool need = (cmask >> ((lane & 31) >> 2)) & 1;
#pragma unroll 1
      for (unsigned spins = 0; spins < SPIN_CAP; ++spins) {
        const unsigned long long e1 = (((unsigned long long)cell.y << 32) | cell.x) + 1ull;
        const bool ok = !need || (cell.z == (unsigned)e1 && cell.w == (unsigned)(e1 >> 32));
        if (__all(ok)) break;
        __builtin_amdgcn_s_sleep(2);
        asm volatile("" ::: "memory");
        cell = __builtin_amdgcn_raw_buffer_load_b128(rsW, W_CELL + (kvh * ROWS + (lane & 31)) * 64, 0, AUX_SC1);
      }
    }
    float wt[2][WAVES], Mr[2], Lr[2];
    int oadr[2], gofs[2];
    if (tb[0] >= 0) {
      while (__hip_atomic_load(reinterpret_cast<int*>(lds + L_CNT), __ATOMIC_RELAXED, __HIP_MEMORY_SCOPE_WORKGROUP) < WAVES * (round + 1)) __builtin_amdgcn_s_sleep(1);
      asm volatile("" ::: "memory");
    }
#pragma unroll
    for (int it = 0; it < 2; ++it) {
      if (tb[it] >= 0) {
        const int row = 4 * tb[it] + ((tid & 127) >> 5);
        const f32x4 m0 = *reinterpret_cast<const f32x4*>(lds + L_MX + row * WAVES * 4), m1 = *reinterpret_cast<const f32x4*>(lds + L_MX + row * WAVES * 4 + 16);
        const f32x4 l0 = *reinterpret_cast<const f32x4*>(lds + L_LS + row * WAVES * 4), l1 = *reinterpret_cast<const f32x4*>(lds + L_LS + row * WAVES * 4 + 16);
        const float M = fmax3(fmax3(m0.x, m0.y, m0.z), fmax3(m0.w, m1.x, m1.y), fmax3(m1.z, m1.w, m1.w)), Ms = (M == -INFINITY) ? 0.f : M;
        float L = 0.f;
#pragma unroll
        for (int ww = 0; ww < WAVES; ++ww) {
          wt[it][ww] = __builtin_amdgcn_exp2f((ww < 4 ? m0[ww & 3] : m1[ww & 3]) - Ms);
          L = fmaf(wt[it][ww], ww < 4 ? l0[ww & 3] : l1[ww & 3], L);
          asm volatile("" : "+v"(wt[it][ww]));
        }
        Mr[it] = __uint_as_float(fix(M)); Lr[it] = __uint_as_float(fix(L));
        oadr[it] = L_O + row * OROW + 16 * (tid & 31);
        gofs[it] = W_PART + (kvh * ROWS + row) * ROW_B;
        asm volatile("" : "+v"(Mr[it]), "+v"(Lr[it]), "+v"(oadr[it]), "+v"(gofs[it]));
      }
    }
    __builtin_amdgcn_s_waitcnt(0x0F70);
    QSTAMP(6);
    BARRIER();
    STAMP(5);
#pragma unroll
    for (int it = 0; it < 2; ++it) {
      if (tb[it] >= 0) {
        f32x4 x[WAVES];
#pragma unroll
        for (int ww = 0; ww < WAVES; ++ww) x[ww] = *reinterpret_cast<const f32x4*>(lds + oadr[it] + ww * WREG);
        if ((tid & 31) == 0) __builtin_amdgcn_raw_buffer_store_b64(u32x2{__float_as_uint(Mr[it]), __float_as_uint(Lr[it])}, rsW, gofs[it], tr[it] * 8, AUX_SC1);
        f32x4 o = x[0] * wt[it][0];
#pragma unroll
        for (int ww = 1; ww < WAVES; ++ww) o += x[ww] * wt[it][ww];
        __builtin_amdgcn_raw_buffer_store_b128(u32x4{fix(o.x), fix(o.y), fix(o.z), fix(o.w)}, rsW, gofs[it] + 16 * (tid & 31), PAY + tr[it] * SLOT_B, AUX_SC1);
      }
    }
  };
  chunk(0);
  if (__builtin_expect(nrounds > 1, 0)) {
#pragma unroll 1
    for (int round = 1; round < nrounds; ++round) { BARRIER(); chunk(round); }
  }
  STAMP(6);
  if (w == WAVES - 1) {
    const int p4 = lane & 31, hf = lane >> 5;
    float M = -INFINITY, L = 0.f;
    f32x4 o = {0.f, 0.f, 0.f, 0.f};
    struct Set { u32x4 x[PG]; u32x2 ml[PG]; };
    auto fetch = [&](Set& s, int k0) __attribute__((always_inline)) {
      asm volatile("" ::: "memory");
#pragma unroll
      for (int i = 0; i < PG; ++i) {
        const int k = k0 + 2 * i + hf, dead = (np - 1 - k) & OOB;
        s.x[i] = __builtin_amdgcn_raw_buffer_load_b128(rsW, (PAY + k * SLOT_B + 16 * p4) | dead, myrow, AUX_SC1);
        s.ml[i] = __builtin_amdgcn_raw_buffer_load_b64(rsW, (k * 8) | dead, myrow, AUX_SC1);
      }
      asm volatile("" ::: "memory");
    };
    auto complete = [&](const Set& s) __attribute__((always_inline)) {
      unsigned top = 0;
#pragma unroll
      for (int i = 0; i < PG; ++i) top = max(max(max(top, s.x[i].x), max(s.x[i].y, s.x[i].z)), max(max(s.x[i].w, s.ml[i].x), s.ml[i].y));
      return __all(top != EMPTY);
    };
    auto absorb = [&](const Set& s, int k0) __attribute__((always_inline)) {
      float mg = M;
#pragma unroll
      for (int i = 0; i < PG; ++i) mg = fmaxf(mg, (k0 + 2 * i + hf >= np) ? -INFINITY : __uint_as_float(s.ml[i].x));
      mg = xh_max(mg);
      const float ms = (mg == -INFINITY) ? 0.f : mg;
      const float al = __builtin_amdgcn_exp2f(M - ms);
      o *= al; L *= al; M = mg;
#pragma unroll
      for (int i = 0; i < PG; ++i) {
        const float wv = (k0 + 2 * i + hf >= np) ? 0.f : __builtin_amdgcn_exp2f(__uint_as_float(s.ml[i].x) - ms);
        o += __builtin_bit_cast(f32x4, s.x[i]) * wv;
        L = fmaf(wv, __uint_as_float(s.ml[i].y), L);
      }
    };
    auto finish = [&]() __attribute__((always_inline)) {
#pragma unroll
      for (int e = 0; e < 4; ++e) o[e] = xh_sum(o[e]);
      const float inv = __builtin_amdgcn_rcpf(xh_sum(L));
      const unsigned pk = pack_bf16((hf ? o[2] : o[0]) * inv, (hf ? o[3] : o[1]) * inv);
      const __amdgpu_buffer_rsrc_t rsO = __builtin_amdgcn_make_buffer_rsrc((void*)out, 0, B * H * D * 2, 0x00020000);
      const int ob = ((crow >> 2) * H + ckvh * 4 + (crow & 3)) * D * 2, ov = (32 * (p4 & 3) + 16 * hf + (p4 >> 2)) * 2;
      __builtin_amdgcn_raw_buffer_store_b16((unsigned short)pk, rsO, ov, ob, OUT_AUX);
      __builtin_amdgcn_raw_buffer_store_b16((unsigned short)(pk >> 16), rsO, ov + 16, ob, OUT_AUX);
      if (lane == 0) __builtin_amdgcn_raw_buffer_store_b64(epoch, rsW, mycell, 0, OUT_AUX);
    };
    STAMP(1);
    for (int k0 = 0; k0 < np; k0 += 2 * PG) {
      Set sa;
      fetch(sa, k0);
#pragma unroll 1
      for (unsigned spins = 0; spins < SPIN_CAP && !complete(sa); ++spins) { __builtin_amdgcn_s_sleep(1); fetch(sa, k0); }
      absorb(sa, k0);
    }
    finish();
    asm volatile(".Lradix_end:");
    STAMP(2);
  }
}
}

extern "C" void kernel_launch(void* const* d_in, const int* in_sizes, int n_in, void* d_out, int out_size, void* d_ws,
                              size_t ws_size, hipStream_t stream) {
  attn_radix<<<dim3(HKV * ROWS), dim3(NTHR), 0, stream>>>((const unsigned char*)d_in[0], (const unsigned char*)d_in[1],
                                                           (const unsigned char*)d_in[2], (const int*)d_in[3],
                                                           (const int*)d_in[4], (unsigned char*)d_out, (unsigned char*)d_ws,
                                                           (unsigned long long)in_sizes[1] * 2ull);
}
```

```cpp
#include <hip/hip_runtime.h>
#include <hip/hip_bf16.h>
#include <stdint.h>

typedef __attribute__((ext_vector_type(8))) __bf16 bf16x8;
typedef __attribute__((ext_vector_type(4))) float f32x4;
typedef __attribute__((ext_vector_type(4))) unsigned u32x4;
typedef __attribute__((ext_vector_type(2))) unsigned u32x2;
typedef __attribute__((ext_vector_type(4))) int i32x4;
typedef __attribute__((ext_vector_type(2))) unsigned short u16x2;
#define LAS __attribute__((address_space(3)))

namespace {
constexpr int B = 8, H = 32, HKV = 8, D = 128, BS = 16, NB = 256, LK = NB * BS;
constexpr int WNUM = 4;
constexpr int NSL = 38;
constexpr int NF = (128 - NSL) / 3;
constexpr int GAP = 3;
constexpr int VFIRST = 2;
constexpr int PG = 7, NEARLY = 2 * PG;
static_assert(NSL + 3 * NF == 128, "four heads share 128 workgroups");
constexpr int WAVES = 8, NTHR = 512;
constexpr int BPW = 4;
constexpr int ROWS = 32;
constexpr int TOK_BYTES = HKV * D * 2;
constexpr int QROW = 272;
constexpr int OROW = 528;
constexpr int WREG = ROWS * OROW;
constexpr int AUX_SC1 = 16, AUX_NT = 2;
constexpr int L_O = 0;
constexpr int L_BT = L_O + WAVES * WREG;
constexpr int L_Q = L_BT + B * NB * 4;
constexpr int L_MX = L_Q + ROWS * QROW;
constexpr int L_LS = L_MX + ROWS * WAVES * 4;
constexpr int L_NS = L_LS + ROWS * WAVES * 4;
constexpr int L_CNT = L_NS + 16;
constexpr int L_END = L_CNT + 16;
constexpr int NSLOT = 96, CELL = 768, PAY = 1024, SLOT_B = 512, ROW_B = PAY + NEARLY * SLOT_B, ROW2_B = (NSLOT - NEARLY) * SLOT_B;
constexpr int W_FAR = HKV * ROWS * ROW_B, W_END = W_FAR + HKV * ROWS * ROW2_B;
__device__ __forceinline__ int slot_off(int grow, int k) { return k < NEARLY ? grow * ROW_B + PAY + k * SLOT_B : W_FAR + grow * ROW2_B + (k - NEARLY) * SLOT_B; }
constexpr unsigned EMPTY = 0xffffffffu;
constexpr unsigned SPIN_CAP = 1u << 20;
constexpr int OOB = (int)0x80000000;

__device__ __forceinline__ float fmax2(float a, float b) { float d; asm("v_max_f32 %0, %1, %2" : "=v"(d) : "v"(a), "v"(b)); return d; }
__device__ __forceinline__ float xg_max(float x) {
  auto s = __builtin_amdgcn_permlane16_swap(__float_as_uint(x), __float_as_uint(x), false, false);
  x = fmax2(__uint_as_float(s[0]), __uint_as_float(s[1]));
  auto t = __builtin_amdgcn_permlane32_swap(__float_as_uint(x), __float_as_uint(x), false, false);
  return fmax2(__uint_as_float(t[0]), __uint_as_float(t[1]));
}
__device__ __forceinline__ float xh_max(float x) {
  auto t = __builtin_amdgcn_permlane32_swap(__float_as_uint(x), __float_as_uint(x), false, false);
  return fmaxf(__uint_as_float(t[0]), __uint_as_float(t[1]));
}
__device__ __forceinline__ float xh_sum(float x) {
  auto t = __builtin_amdgcn_permlane32_swap(__float_as_uint(x), __float_as_uint(x), false, false);
  return __uint_as_float(t[0]) + __uint_as_float(t[1]);
}
__device__ __forceinline__ unsigned pack_bf16(float lo, float hi) {
  typedef __attribute__((ext_vector_type(2))) __bf16 bf16x2;
  bf16x2 v = {(__bf16)lo, (__bf16)hi};
  return __builtin_bit_cast(unsigned, v);
}
__device__ __forceinline__ f32x4 mfma(u32x4 a, u32x4 b, f32x4 c) {
  return __builtin_amdgcn_mfma_f32_16x16x32_bf16(__builtin_bit_cast(bf16x8, a), __builtin_bit_cast(bf16x8, b), c, 0, 0, 0);
}
template <int CTRL> __device__ __forceinline__ int dpp0(int x) {
  return __builtin_amdgcn_update_dpp(0, x, CTRL, 0xf, 0xf, true);
}
__device__ __forceinline__ unsigned fix(float x) { return min(__float_as_uint(x), EMPTY - 1u); }
__device__ __forceinline__ unsigned ff1(unsigned long long m) { unsigned d; asm("s_ff1_i32_b64 %0, %1" : "=s"(d) : "s"(m)); return d; }
__device__ __forceinline__ unsigned pkmin(unsigned a, unsigned b) {
  return __builtin_bit_cast(unsigned, __builtin_elementwise_min(__builtin_bit_cast(u16x2, a), __builtin_bit_cast(u16x2, b)));
}
#define PIN_S(x) asm volatile("" : "+s"(x))
#define BARRIER() asm volatile("s_waitcnt lgkmcnt(0)\n\ts_barrier" ::: "memory")

__global__ void __launch_bounds__(NTHR, 2)
attn_radix(const unsigned char* __restrict__ q, const unsigned char* __restrict__ kc, const unsigned char* __restrict__ vc,
           const int* __restrict__ bt, const int* __restrict__ ctx, unsigned char* __restrict__ out,
           unsigned char* __restrict__ ws, unsigned long long kv_bytes) {
  __shared__ __attribute__((aligned(16))) unsigned char lds[L_END];
  const int tid = threadIdx.x, lane = tid & 63, w = __builtin_amdgcn_readfirstlane(tid >> 6);
  const int g = lane >> 4, r = lane & 15;
  asm volatile("" :: "s"(q), "s"(kc), "s"(vc), "s"(bt), "s"(ctx), "s"(out), "s"(ws), "s"(kv_bytes));
  const int craw = ctx[lane & 7];
  const i32x4 bz = *reinterpret_cast<const i32x4*>(reinterpret_cast<const unsigned char*>(bt) + lane * 16);
  const i32x4 ba = *reinterpret_cast<const i32x4*>(reinterpret_cast<const unsigned char*>(bt) + tid * 16);
  __builtin_amdgcn_sched_barrier(0);
  {
    unsigned lo, hi, len;
    asm volatile("s_getpc_b64 s[6:7]\n.Lradix_pc:\n\ts_and_b32 %0, s6, 0xffffff00\n\ts_mov_b32 %1, s7\n\ts_mov_b32 %2, .Lradix_end-.Lradix_pc"
                 : "=s"(lo), "=s"(hi), "=s"(len) :: "s6", "s7", "scc");
    const __amdgpu_buffer_rsrc_t rsC = __builtin_amdgcn_make_buffer_rsrc((void*)(((unsigned long long)hi << 32) | lo), 0, len, 0x00020000);
    __builtin_amdgcn_raw_ptr_buffer_load_lds(rsC, (LAS void*)(lds + L_O + w * WREG + 16384), 4, tid * 128, 0, 0, 0);
  }
  const int ak = (int)((size_t)kc >> 8) & 3, av = (int)((size_t)vc >> 8) & 3;
  const bool skew = ak == av;
  const int slow = (1 - ak) & 3, nsl = skew ? NSL : 32, nf = skew ? NF : 32;
  const int grp = blockIdx.x & 7;
  int kvh, wg, slowp;
  {
    const int id = grp * 32 + (blockIdx.x >> 3), rem = id & 127, t = rem - nsl;
    const int qd = (t * (skew ? (65536 + NF - 1) / NF : 2048)) >> 16;
    kvh = (slow + (id >> 7) * 4 + (t < 0 ? 0 : 1 + qd)) & 7;
    wg = t < 0 ? rem : t - qd * nf;
    slowp = t < 0;
  }
  const u32x4 qv = *reinterpret_cast<const u32x4*>(q + (w * H + kvh * 4) * D * 2 + lane * 16);
  const int c0 = ctx[r >> 2], c1 = ctx[4 + (r >> 2)];
  const int ckvh = (slow + 4 + grp) & 7, crow = blockIdx.x >> 3;
  const __amdgpu_buffer_rsrc_t rsK = __builtin_amdgcn_make_buffer_rsrc((void*)kc, 0, (unsigned)kv_bytes, 0x00020000);
  const __amdgpu_buffer_rsrc_t rsV = __builtin_amdgcn_make_buffer_rsrc((void*)vc, 0, (unsigned)kv_bytes, 0x00020000);
  const __amdgpu_buffer_rsrc_t rsW = __builtin_amdgcn_make_buffer_rsrc((void*)ws, 0, W_END, 0x00020000);
  const int grow = ckvh * ROWS + crow, myrow = grow * ROW_B, mycell = myrow + CELL;
  const u32x4 e4 = {EMPTY, EMPTY, EMPTY, EMPTY};
  const __amdgpu_buffer_rsrc_t rsO = __builtin_amdgcn_make_buffer_rsrc((void*)out, 0, B * H * D * 2, 0x00020000);
  const int ob = ((crow >> 2) * H + ckvh * 4 + (crow & 3)) * D * 2;
  u32x2 epoch = __builtin_amdgcn_raw_buffer_load_b64(rsW, mycell, 0, AUX_SC1);
  int khead = kvh * 256, cv = (lane & 1) ? nsl : nf;
  const int xr = (r ^ g) * 16;
  int kofs[4], vofs = 4 * g * TOK_BYTES + r * 16;
#pragma unroll
  for (int i = 0; i < 4; ++i) kofs[i] = (4 * i + g) * TOK_BYTES + (xr ^ (64 * i));
  PIN_S(khead); PIN_S(slowp); PIN_S(wg);
  asm volatile("" : "+v"(kofs[0]), "+v"(kofs[1]), "+v"(kofs[2]), "+v"(kofs[3]), "+v"(vofs), "+v"(cv));
  const int nblk = (((craw <= 0 || craw > LK) ? LK : craw) + BS - 1) >> 4;
  int cum = nblk + dpp0<0x111>(nblk); cum += dpp0<0x112>(cum); cum += dpp0<0x114>(cum);
  cum += (r >= 4) ? GAP : 0;
  int mn = 256 - nblk; mn = max(mn, dpp0<0x111>(mn)); mn = max(mn, dpp0<0x112>(mn)); mn = max(mn, dpp0<0x114>(mn));
  int minblk = 256 - __builtin_amdgcn_readlane(mn, 7), total = __builtin_amdgcn_readlane(cum, 7);
  int hi_a = r < 8 ? cum : 0, hi_b = r < 8 ? -r : (r == 8), lo_a = r < 8 ? cum - nblk : 0, lo_b = r < 8 ? 1 - r : 0;
  int btrow = L_BT + (r < 8 ? r : 0) * NB * 4 - 4 * lo_a, bt_b = r < 8 ? 4 * r : 0;
  PIN_S(minblk); PIN_S(total);
  asm volatile("" : "+v"(hi_a), "+v"(hi_b), "+v"(lo_a), "+v"(lo_b), "+v"(btrow), "+v"(bt_b));
  __builtin_amdgcn_sched_barrier(0);
  {
    const unsigned n0 = 4 * ff1(__ballot(ba.x != bz.x)), n1 = 4 * ff1(__ballot(ba.y != bz.y)) + 1, n2 = 4 * ff1(__ballot(ba.z != bz.z)) + 2,
                   n3 = 4 * ff1(__ballot(ba.w != bz.w)) + 3;
    const unsigned ns = min(min(n0, n1), min(n2, n3));
    *reinterpret_cast<unsigned short*>(lds + L_NS + 2 * w) = (unsigned short)ns;
    *reinterpret_cast<i32x4*>(lds + L_BT + tid * 16) = ba;
    if (tid == 0) *reinterpret_cast<int*>(lds + L_CNT) = 0;
  }
  if (w == 0) {
#pragma unroll
    for (int k = 0; k < NEARLY; k += 2) __builtin_amdgcn_raw_buffer_store_b128(e4, rsW, lane * 16, myrow + PAY + k * SLOT_B, AUX_SC1);
    if (lane < CELL / 16) __builtin_amdgcn_raw_buffer_store_b128(e4, rsW, lane * 16, myrow, AUX_SC1);
  }
  BARRIER();
  int NSv;
  {
    const u32x4 n = *reinterpret_cast<const u32x4*>(lds + L_NS);
    const unsigned m = pkmin(pkmin(n.x, n.y), pkmin(n.z, n.w));
    NSv = min((int)min(m & 0xffffu, m >> 16), minblk);
  }
  const int seg_hi = __mul24(hi_b, NSv) + hi_a, seg_lo = __mul24(lo_b, NSv) + lo_a, btadr = __mul24(bt_b, NSv) + btrow;
  struct Plan { int rounds, nw8; float ss, sp; };
  Plan pv;
  {
    const int P = total - 8 * NSv, ns_min = NSv > 0 ? (NSv >> 5) + 1 : 0, np_min = (P >> 5) + 1;
    pv.rounds = ns_min + np_min <= cv ? 1 : ns_min + np_min <= 2 * cv ? 2 : 3;
    const int nc = __mul24(cv, pv.rounds);
    const float fs = (float)NSv, fp = (float)P, fa = fs * (1.f + WNUM * 0.125f);
    const int nw = min(max((int)(fa * (float)nc * __builtin_amdgcn_rcpf(fa + fp) + 0.5f), ns_min), nc - np_min);
    constexpr float UP = 0.125f * (1.f + 1.f / 1048576.f);
    pv.nw8 = 8 * nw; pv.ss = fs * UP * __builtin_amdgcn_rcpf((float)max(nw, 1)); pv.sp = fp * UP * __builtin_amdgcn_rcpf((float)(nc - nw));
  }
  auto plan_of = [&](int odd) {
    return Plan{__builtin_amdgcn_readlane(pv.rounds, odd), __builtin_amdgcn_readlane(pv.nw8, odd),
                __int_as_float(__builtin_amdgcn_readlane(__float_as_int(pv.ss), odd)), __int_as_float(__builtin_amdgcn_readlane(__float_as_int(pv.sp), odd))};
  };
  auto start = [&](const Plan& p, int u) { const bool sh = u < p.nw8; return (sh ? 0 : NSv) + (int)((float)(sh ? u : u - p.nw8) * (sh ? p.ss : p.sp)); };
  auto producers = [&](const Plan& p, int b, int lim) {
    const int lo = __builtin_amdgcn_readlane(seg_lo, b), hi = __builtin_amdgcn_readlane(seg_hi, b);
    const int a = start(p, 8 * lane), e = start(p, 8 * lane + 8);
    int cnt = __builtin_popcountll(__ballot(lane < lim && a < e && (8 * lane < p.nw8 || (a < hi && e > lo))));
    if (lim > 64) {
      const int a2 = start(p, 8 * lane + 512), e2 = start(p, 8 * lane + 520);
      cnt += __builtin_popcountll(__ballot(lane + 64 < lim && a2 < e2 && (8 * lane + 512 < p.nw8 || (a2 < hi && e2 > lo))));
    }
    return cnt;
  };
  const Plan pl = plan_of(slowp);
  int np = 0;
  constexpr float C2 = 0.088388347648318440f * 1.4426950408889634f;

  auto chunk = [&](const int round) __attribute__((always_inline)) {
    const int v = wg * pl.rounds + round;
    const int ws0 = start(pl, 8 * v + w), ws1 = start(pl, 8 * v + w + 1);
    const int f = ws0 + g;
    const bool in = (f >= seg_lo) & (f < min(seg_hi, ws1));
    const int ph = *reinterpret_cast<const int*>(lds + (in ? btadr + 4 * f : L_BT));
    const unsigned long long im = __ballot(in);
    const int sov = (ph << 15) + khead;
    int so[BPW];
    auto count_row = [&]() {
      const Plan cp = plan_of((grp & 3) == 0);
      np = producers(cp, crow >> 2, ((grp & 3) == 0 ? nsl : nf) * cp.rounds);
      const unsigned long long e1 = (((unsigned long long)epoch.y << 32) | epoch.x) + 1ull;
      epoch = u32x2{(unsigned)e1, (unsigned)(e1 >> 32)};
    };
    if (round == 0 && w == 0) {
      count_row();
      for (int k = NEARLY; k < np; k += 2)
        if (k + (lane >> 5) < np) __builtin_amdgcn_raw_buffer_store_b128(e4, rsW, lane * 16, slot_off(grow, k), AUX_SC1);
      asm volatile("s_waitcnt vmcnt(0)" ::: "memory");
      if (lane == 0) __builtin_amdgcn_raw_buffer_store_b64(epoch, rsW, mycell + 8, 0, AUX_SC1);
    }
#pragma unroll
    for (int t = 0; t < BPW; ++t) {
      const unsigned long long mt = im & (0xffffull << (16 * t));
      const int sx = __builtin_amdgcn_readlane(sov, ff1(mt) & 63);
      so[t] = mt ? sx : OOB;
#pragma unroll
      for (int i = 0; i < 4; ++i)
        __builtin_amdgcn_raw_ptr_buffer_load_lds(rsK, (LAS void*)(lds + L_O + w * WREG + t * 4096 + i * 1024), 16, kofs[i], so[t], 0, AUX_NT);
    }
    int jb[BPW], mk[BPW];
    {
      int pk = in ? ((r == 8) ? (f | 0xff0000) : ((f - seg_lo + NSv) | (0x10000 << (r & 7)))) : 0;
      pk |= dpp0<0x111>(pk); pk |= dpp0<0x112>(pk); pk |= dpp0<0x114>(pk); pk |= dpp0<0x118>(pk);
#pragma unroll
      for (int t = 0; t < BPW; ++t) { const int p = __builtin_amdgcn_readlane(pk, 16 * t + 15); jb[t] = p & 0xffff; mk[t] = p >> 16; }
    }
    const int wmask = mk[0] | mk[1] | mk[2] | mk[3];
    const int cl[2] = {(c0 <= 0 || c0 > LK) ? LK : c0, (c1 <= 0 || c1 > LK) ? LK : c1};
    int lim[2][BPW];
#pragma unroll
    for (int rt = 0; rt < 2; ++rt)
#pragma unroll
      for (int t = 0; t < BPW; ++t) { lim[rt][t] = ((mk[t] >> (4 * rt + (r >> 2))) & 1) ? cl[rt] - jb[t] * BS - 4 * g : 0; asm volatile("" : "+v"(lim[rt][t])); }
    const int s0 = start(pl, 8 * v), s1 = start(pl, 8 * v + 8);
    const int cmask = (int)__ballot(lane < 8 && s0 < s1 && (8 * v < pl.nw8 || (s0 < seg_hi && s1 > seg_lo)));
    int tb[2], tr[2];
    {
      int rem = cmask;
      for (int k = 0; k < (w >> 1); ++k) rem &= rem - 1;
#pragma unroll
      for (int it = 0; it < 2; ++it) {
        tb[it] = rem ? __builtin_ctz(rem) : -1;
        tr[it] = rem ? producers(pl, tb[it], v) : 0;
        rem &= rem - 1; rem &= rem - 1; rem &= rem - 1; rem &= rem - 1;
      }
    }
    bool plain[2];
#pragma unroll
    for (int rt = 0; rt < 2; ++rt)
      plain[rt] = __all((min(min(lim[rt][0], lim[rt][1]), min(lim[rt][2], lim[rt][3])) | (((cmask >> (4 * rt + (r >> 2))) & 1) ? 0 : 4)) >= 4);
    if (round == 0) {
      if (w == WAVES - 1) count_row();
      const bool dead = __builtin_amdgcn_readlane(craw, w) <= 0;
      *reinterpret_cast<u32x4*>(lds + L_Q + (w * 4 + g) * QROW + r * 16) = dead ? u32x4{0u, 0u, 0u, 0u} : qv;
    }
    __builtin_amdgcn_sched_barrier(0);
    BARRIER();
    u32x4 cell = {0u, 0u, 1u, 0u};
    if (w == 0) cell = __builtin_amdgcn_raw_buffer_load_b128(rsW, (kvh * ROWS + (lane & 31)) * ROW_B + CELL, 0, AUX_SC1);
    __builtin_amdgcn_sched_barrier(0);
    u32x4 vr[BPW][4];
#pragma unroll
    for (int t = 0; t < VFIRST; ++t)
#pragma unroll
      for (int i = 0; i < 4; ++i) vr[t][i] = __builtin_amdgcn_raw_buffer_load_b128(rsV, vofs + i * TOK_BYTES, so[t], AUX_NT);
    __builtin_amdgcn_sched_barrier(0);
    asm volatile("s_waitcnt vmcnt(%0)" :: "i"(4 * VFIRST) : "memory");
    u32x4 pf[2][2];
#pragma unroll
    for (int rt = 0; rt < 2; ++rt) {
      float m = -INFINITY, l = 0.f;
      pf[rt][0] = u32x4{0, 0, 0, 0}; pf[rt][1] = u32x4{0, 0, 0, 0};
      if ((wmask >> (4 * rt)) & 0xf) {
        u32x4 qf[4], kf[BPW][4];
        f32x4 s[BPW];
#pragma unroll
        for (int i = 0; i < 4; ++i) {
          qf[i] = *reinterpret_cast<const u32x4*>(lds + L_Q + (16 * rt + r) * QROW + 64 * i + 16 * g);
#pragma unroll
          for (int t = 0; t < BPW; ++t) kf[t][i] = *reinterpret_cast<const u32x4*>(lds + L_O + w * WREG + t * 4096 + r * 256 + (xr ^ (64 * i)));
        }
#pragma unroll
        for (int t = 0; t < BPW; ++t) s[t] = f32x4{0.f, 0.f, 0.f, 0.f};
#pragma unroll
        for (int i = 0; i < 4; ++i)
#pragma unroll
          for (int t = 0; t < BPW; ++t) s[t] = mfma(kf[t][i], qf[i], s[t]);
        if (!plain[rt]) {
#pragma unroll
          for (int t = 0; t < BPW; ++t)
#pragma unroll
            for (int e = 0; e < 4; ++e) s[t][e] = (e < lim[rt][t]) ? s[t][e] : -INFINITY;
        }
#pragma unroll
        for (int t = 0; t < BPW; ++t) m = fmaxf(fmaxf(fmaxf(fmaxf(m, s[t][0]), s[t][1]), s[t][2]), s[t][3]);
        m = xg_max(m) * C2;
        const float ms = (m == -INFINITY) ? 0.f : -m;
#pragma unroll
        for (int t = 0; t < BPW; ++t) {
          f32x4 p;
#pragma unroll
          for (int e = 0; e < 4; ++e) p[e] = __builtin_amdgcn_exp2f(fmaf(s[t][e], C2, ms));
          pf[rt][t >> 1][2 * (t & 1)] = pack_bf16(p[0], p[1]);
          pf[rt][t >> 1][2 * (t & 1) + 1] = pack_bf16(p[2], p[3]);
        }
        const u32x4 ones = {0x3f803f80u, 0x3f803f80u, 0x3f803f80u, 0x3f803f80u};
        l = mfma(ones, pf[rt][1], mfma(ones, pf[rt][0], f32x4{0.f, 0.f, 0.f, 0.f}))[0];
      }
      if (g == 0) {
        *reinterpret_cast<float*>(lds + L_MX + ((16 * rt + r) * WAVES + w) * 4) = m;
        *reinterpret_cast<float*>(lds + L_LS + ((16 * rt + r) * WAVES + w) * 4) = l;
      }
    }
    __builtin_amdgcn_sched_barrier(0);
#pragma unroll
    for (int t = VFIRST; t < BPW; ++t)
#pragma unroll
      for (int i = 0; i < 4; ++i) vr[t][i] = __builtin_amdgcn_raw_buffer_load_b128(rsV, vofs + i * TOK_BYTES, so[t], AUX_NT);
    __builtin_amdgcn_sched_barrier(0);
    asm volatile("s_waitcnt lgkmcnt(0)" ::: "memory");
    if (lane == 0) __hip_atomic_fetch_add(reinterpret_cast<int*>(lds + L_CNT), 1, __ATOMIC_RELAXED, __HIP_MEMORY_SCOPE_WORKGROUP);
    f32x4 acc[2][8];
#pragma unroll
    for (int j = 0; j < 8; ++j) { acc[0][j] = f32x4{0.f, 0.f, 0.f, 0.f}; acc[1][j] = f32x4{0.f, 0.f, 0.f, 0.f}; }
#pragma unroll
    for (int pr = 0; pr < 2; ++pr) {
      u32x4 vt[8];
#pragma unroll
      for (int c = 0; c < 4; ++c) {
#pragma unroll
        for (int j = 0; j < 8; ++j)
          vt[j][c] = __builtin_amdgcn_perm(vr[2 * pr + (c >> 1)][2 * (c & 1) + 1][j >> 1], vr[2 * pr + (c >> 1)][2 * (c & 1)][j >> 1], (j & 1) ? 0x07060302u : 0x05040100u);
        __builtin_amdgcn_sched_barrier(0);
      }
      if (wmask & 0x0f) {
#pragma unroll
        for (int j = 0; j < 8; ++j) acc[0][j] = mfma(vt[j], pf[0][pr], acc[0][j]);
      }
      if (wmask & 0xf0) {
#pragma unroll
        for (int j = 0; j < 8; ++j) acc[1][j] = mfma(vt[j], pf[1][pr], acc[1][j]);
      }
    }
#pragma unroll
    for (int rt = 0; rt < 2; ++rt) {
      if ((cmask >> (4 * rt + (r >> 2))) & 1) {
#pragma unroll
        for (int j = 0; j < 8; ++j) *reinterpret_cast<f32x4*>(lds + L_O + w * WREG + (16 * rt + r) * OROW + (4 * j + g) * 16) = acc[rt][j];
      }
    }
    if (w == 0) {
      const bool need = (cmask >> ((lane & 31) >> 2)) & 1;
#pragma unroll 1
      for (unsigned spins = 0; spins < SPIN_CAP; ++spins) {
        const unsigned long long e1 = (((unsigned long long)cell.y << 32) | cell.x) + 1ull;
        const bool ok = !need || (cell.z == (unsigned)e1 && cell.w == (unsigned)(e1 >> 32));
        if (__all(ok)) break;
        __builtin_amdgcn_s_sleep(2);
        asm volatile("" ::: "memory");
        cell = __builtin_amdgcn_raw_buffer_load_b128(rsW, (kvh * ROWS + (lane & 31)) * ROW_B + CELL, 0, AUX_SC1);
      }
    }
    float wt[2][WAVES], Mr[2], Lr[2];
    int oadr[2], gofs[2], gml[2];
    if (tb[0] >= 0) {
      while (__hip_atomic_load(reinterpret_cast<int*>(lds + L_CNT), __ATOMIC_RELAXED, __HIP_MEMORY_SCOPE_WORKGROUP) < WAVES * (round + 1)) __builtin_amdgcn_s_sleep(1);
      asm volatile("" ::: "memory");
    }
#pragma unroll
    for (int it = 0; it < 2; ++it) {
      if (tb[it] >= 0) {
        const int row = 4 * tb[it] + ((tid & 127) >> 5);
        const f32x4 m0 = *reinterpret_cast<const f32x4*>(lds + L_MX + row * WAVES * 4), m1 = *reinterpret_cast<const f32x4*>(lds + L_MX + row * WAVES * 4 + 16);
        const f32x4 l0 = *reinterpret_cast<const f32x4*>(lds + L_LS + row * WAVES * 4), l1 = *reinterpret_cast<const f32x4*>(lds + L_LS + row * WAVES * 4 + 16);
        const float M = fmaxf(fmaxf(fmaxf(fmaxf(fmaxf(fmaxf(fmaxf(m0.x, m0.y), m0.z), m0.w), m1.x), m1.y), m1.z), m1.w), Ms = (M == -INFINITY) ? 0.f : M;
        float L = 0.f;
#pragma unroll
        for (int ww = 0; ww < WAVES; ++ww) {
          wt[it][ww] = __builtin_amdgcn_exp2f((ww < 4 ? m0[ww & 3] : m1[ww & 3]) - Ms);
          L = fmaf(wt[it][ww], ww < 4 ? l0[ww & 3] : l1[ww & 3], L);
          asm volatile("" : "+v"(wt[it][ww]));
        }
        Mr[it] = __uint_as_float(fix(M)); Lr[it] = __uint_as_float(fix(L));
        oadr[it] = L_O + row * OROW + 16 * (tid & 31);
        gml[it] = (kvh * ROWS + row) * ROW_B + tr[it] * 8;
        gofs[it] = slot_off(kvh * ROWS + row, tr[it]) + 16 * (tid & 31);
        asm volatile("" : "+v"(Mr[it]), "+v"(Lr[it]), "+v"(oadr[it]), "+v"(gofs[it]), "+v"(gml[it]));
      }
    }
    __builtin_amdgcn_s_waitcnt(0x0F70);
    BARRIER();
#pragma unroll
    for (int it = 0; it < 2; ++it) {
      if (tb[it] >= 0) {
        f32x4 x[WAVES];
#pragma unroll
        for (int ww = 0; ww < WAVES; ++ww) x[ww] = *reinterpret_cast<const f32x4*>(lds + oadr[it] + ww * WREG);
        if ((tid & 31) == 0) __builtin_amdgcn_raw_buffer_store_b64(u32x2{__float_as_uint(Mr[it]), __float_as_uint(Lr[it])}, rsW, gml[it], 0, AUX_SC1);
        f32x4 o = x[0] * wt[it][0];
#pragma unroll
        for (int ww = 1; ww < WAVES; ++ww) o += x[ww] * wt[it][ww];
        __builtin_amdgcn_raw_buffer_store_b128(u32x4{fix(o.x), fix(o.y), fix(o.z), fix(o.w)}, rsW, gofs[it], 0, AUX_SC1);
      }
    }
  };
  chunk(0);
  const int nrounds = pl.rounds;
  if (__builtin_expect(nrounds > 1, 0)) {
#pragma unroll 1
    for (int round = 1; round < nrounds; ++round) { BARRIER(); chunk(round); }
  }
  if (w == WAVES - 1) {
    const int p4 = lane & 31, hf = lane >> 5;
    float M = -INFINITY, L = 0.f;
    f32x4 o = {0.f, 0.f, 0.f, 0.f};
    struct Set { u32x4 x[PG]; u32x2 ml[PG]; };
    auto fetch = [&](Set& s, int k0) __attribute__((always_inline)) {
      asm volatile("" ::: "memory");
#pragma unroll
      for (int i = 0; i < PG; ++i) {
        const int k = k0 + 2 * i + hf, dead = (np - 1 - k) & OOB;
        s.x[i] = __builtin_amdgcn_raw_buffer_load_b128(rsW, (2 * i * SLOT_B + 16 * lane) | dead, slot_off(grow, k0), AUX_SC1);
        s.ml[i] = __builtin_amdgcn_raw_buffer_load_b64(rsW, (k * 8) | dead, myrow, AUX_SC1);
      }
      asm volatile("" ::: "memory");
    };
    auto complete = [&](const Set& s) __attribute__((always_inline)) {
      unsigned top = 0;
#pragma unroll
      for (int i = 0; i < PG; ++i) top = max(max(max(top, s.x[i].x), max(s.x[i].y, s.x[i].z)), max(max(s.x[i].w, s.ml[i].x), s.ml[i].y));
      return __all(top != EMPTY);
    };
    auto absorb = [&](const Set& s, int k0) __attribute__((always_inline)) {
      float mg = M;
#pragma unroll
      for (int i = 0; i < PG; ++i) mg = fmaxf(mg, (k0 + 2 * i + hf >= np) ? -INFINITY : __uint_as_float(s.ml[i].x));
      mg = xh_max(mg);
      const float ms = (mg == -INFINITY) ? 0.f : mg;
      const float al = __builtin_amdgcn_exp2f(M - ms);
      o *= al; L *= al; M = mg;
#pragma unroll
      for (int i = 0; i < PG; ++i) {
        const float wv = (k0 + 2 * i + hf >= np) ? 0.f : __builtin_amdgcn_exp2f(__uint_as_float(s.ml[i].x) - ms);
        o += __builtin_bit_cast(f32x4, s.x[i]) * wv;
        L = fmaf(wv, __uint_as_float(s.ml[i].y), L);
      }
    };
    auto finish = [&]() __attribute__((always_inline)) {
#pragma unroll
      for (int e = 0; e < 4; ++e) o[e] = xh_sum(o[e]);
      const float inv = __builtin_amdgcn_rcpf(xh_sum(L));
      const unsigned pk = pack_bf16((hf ? o[2] : o[0]) * inv, (hf ? o[3] : o[1]) * inv);
      const int ov = (32 * (p4 & 3) + 16 * hf + (p4 >> 2)) * 2;
      __builtin_amdgcn_raw_buffer_store_b16((unsigned short)pk, rsO, ov, ob, 0);
      __builtin_amdgcn_raw_buffer_store_b16((unsigned short)(pk >> 16), rsO, ov + 16, ob, 0);
      if (lane == 0) __builtin_amdgcn_raw_buffer_store_b64(epoch, rsW, mycell, 0, AUX_SC1);
    };
    for (int k0 = 0; k0 < np; k0 += 2 * PG) {
      Set sa;
      fetch(sa, k0);
#pragma unroll 1
      for (unsigned spins = 0; spins < SPIN_CAP && !complete(sa); ++spins) { __builtin_amdgcn_s_sleep(1); fetch(sa, k0); }
      absorb(sa, k0);
    }
    finish();
    asm volatile(".Lradix_end:");
  }
}
}

extern "C" void kernel_launch(void* const* d_in, const int* in_sizes, int n_in, void* d_out, int out_size, void* d_ws,
                              size_t ws_size, hipStream_t stream) {
  if (n_in != 5 || out_size != B * H * D || ws_size < (size_t)W_END) return;
  attn_radix<<<dim3(HKV * ROWS), dim3(NTHR), 0, stream>>>((const unsigned char*)d_in[0], (const unsigned char*)d_in[1],
                                                           (const unsigned char*)d_in[2], (const int*)d_in[3],
                                                           (const int*)d_in[4], (unsigned char*)d_out, (unsigned char*)d_ws,
                                                           (unsigned long long)in_sizes[1] * 2ull);
}
```

```cpp
#include <hip/hip_runtime.h>
#include <hip/hip_bf16.h>
#include <stdint.h>

typedef __attribute__((ext_vector_type(8))) __bf16 bf16x8;
typedef __attribute__((ext_vector_type(4))) float f32x4;
typedef __attribute__((ext_vector_type(4))) unsigned u32x4;
typedef __attribute__((ext_vector_type(2))) unsigned u32x2;
typedef __attribute__((ext_vector_type(4))) int i32x4;
typedef __attribute__((ext_vector_type(2))) unsigned short u16x2;
#define LAS __attribute__((address_space(3)))

namespace {
constexpr int B = 8, H = 32, HKV = 8, D = 128, BS = 16, NB = 256, LK = NB * BS;
constexpr int WNUM = 4;
constexpr int NSL = 38;
constexpr int NF = (128 - NSL) / 3;
constexpr int GAP = 3;
constexpr int VFIRST = 2;
constexpr int PG = 7, NEARLY = 2 * PG;
static_assert(NSL + 3 * NF == 128, "four heads share 128 workgroups");
constexpr int WAVES = 8, NTHR = 512;
constexpr int BPW = 4;
constexpr int ROWS = 32;
constexpr int TOK_BYTES = HKV * D * 2;
constexpr int QROW = 272;
constexpr int OROW = 528;
constexpr int WREG = ROWS * OROW;
constexpr int AUX_SC1 = 16, AUX_NT = 2;
constexpr int L_O = 0;
constexpr int L_BT = L_O + WAVES * WREG;
constexpr int L_Q = L_BT + B * NB * 4;
constexpr int L_MX = L_Q + ROWS * QROW;
constexpr int L_LS = L_MX + ROWS * WAVES * 4;
constexpr int L_NS = L_LS + ROWS * WAVES * 4;
constexpr int L_CNT = L_NS + 16;
constexpr int L_END = L_CNT + 16;
constexpr int NSLOT = 96, CELL = 768, PAY = 1024, SLOT_B = 512, ROW_B = PAY + NEARLY * SLOT_B, ROW2_B = (NSLOT - NEARLY) * SLOT_B;
constexpr int W_FAR = HKV * ROWS * ROW_B, W_END = W_FAR + HKV * ROWS * ROW2_B;
__device__ __forceinline__ int slot_off(int grow, int k) { return k < NEARLY ? grow * ROW_B + PAY + k * SLOT_B : W_FAR + grow * ROW2_B + (k - NEARLY) * SLOT_B; }
constexpr unsigned EMPTY = 0xffffffffu;
constexpr unsigned SPIN_CAP = 1u << 20;
constexpr int OOB = (int)0x80000000;

__device__ __forceinline__ float fmax2(float a, float b) { float d; asm("v_max_f32 %0, %1, %2" : "=v"(d) : "v"(a), "v"(b)); return d; }
__device__ __forceinline__ float xg_max(float x) {
  auto s = __builtin_amdgcn_permlane16_swap(__float_as_uint(x), __float_as_uint(x), false, false);
  x = fmax2(__uint_as_float(s[0]), __uint_as_float(s[1]));
  auto t = __builtin_amdgcn_permlane32_swap(__float_as_uint(x), __float_as_uint(x), false, false);
  return fmax2(__uint_as_float(t[0]), __uint_as_float(t[1]));
}
__device__ __forceinline__ float xh_max(float x) {
  auto t = __builtin_amdgcn_permlane32_swap(__float_as_uint(x), __float_as_uint(x), false, false);
  return fmaxf(__uint_as_float(t[0]), __uint_as_float(t[1]));
}
__device__ __forceinline__ float xh_sum(float x) {
  auto t = __builtin_amdgcn_permlane32_swap(__float_as_uint(x), __float_as_uint(x), false, false);
  return __uint_as_float(t[0]) + __uint_as_float(t[1]);
}
__device__ __forceinline__ unsigned pack_bf16(float lo, float hi) {
  typedef __attribute__((ext_vector_type(2))) __bf16 bf16x2;
  bf16x2 v = {(__bf16)lo, (__bf16)hi};
  return __builtin_bit_cast(unsigned, v);
}
__device__ __forceinline__ f32x4 mfma(u32x4 a, u32x4 b, f32x4 c) {
  return __builtin_amdgcn_mfma_f32_16x16x32_bf16(__builtin_bit_cast(bf16x8, a), __builtin_bit_cast(bf16x8, b), c, 0, 0, 0);
}
template <int CTRL> __device__ __forceinline__ int dpp0(int x) {
  return __builtin_amdgcn_update_dpp(0, x, CTRL, 0xf, 0xf, true);
}
__device__ __forceinline__ unsigned fix(float x) { return min(__float_as_uint(x), EMPTY - 1u); }
__device__ __forceinline__ unsigned ff1(unsigned long long m) { unsigned d; asm("s_ff1_i32_b64 %0, %1" : "=s"(d) : "s"(m)); return d; }
__device__ __forceinline__ unsigned pkmin(unsigned a, unsigned b) {
  return __builtin_bit_cast(unsigned, __builtin_elementwise_min(__builtin_bit_cast(u16x2, a), __builtin_bit_cast(u16x2, b)));
}
#define PIN_S(x) asm volatile("" : "+s"(x))
#define BARRIER() asm volatile("s_waitcnt lgkmcnt(0)\n\ts_barrier" ::: "memory")

__global__ void __launch_bounds__(NTHR, 2)
attn_radix(const unsigned char* __restrict__ q, const unsigned char* __restrict__ kc, const unsigned char* __restrict__ vc,
           const int* __restrict__ bt, const int* __restrict__ ctx, unsigned char* __restrict__ out,
           unsigned char* __restrict__ ws, unsigned long long kv_bytes) {
  __shared__ __attribute__((aligned(16))) unsigned char lds[L_END];
  const int tid = threadIdx.x, lane = tid & 63, w = __builtin_amdgcn_readfirstlane(tid >> 6);
  const int g = lane >> 4, r = lane & 15;
  asm volatile("" :: "s"(q), "s"(kc), "s"(vc), "s"(bt), "s"(ctx), "s"(out), "s"(ws), "s"(kv_bytes));
  const int craw = ctx[lane & 7];
  const i32x4 bz = *reinterpret_cast<const i32x4*>(reinterpret_cast<const unsigned char*>(bt) + lane * 16);
  const i32x4 ba = *reinterpret_cast<const i32x4*>(reinterpret_cast<const unsigned char*>(bt) + tid * 16);
  __builtin_amdgcn_sched_barrier(0);
  {
    unsigned lo, hi, len;
    asm volatile("s_getpc_b64 s[6:7]\n.Lradix_pc:\n\ts_and_b32 %0, s6, 0xffffff00\n\ts_mov_b32 %1, s7\n\ts_mov_b32 %2, .Lradix_end-.Lradix_pc"
                 : "=s"(lo), "=s"(hi), "=s"(len) :: "s6", "s7", "scc");
    const __amdgpu_buffer_rsrc_t rsC = __builtin_amdgcn_make_buffer_rsrc((void*)(((unsigned long long)hi << 32) | lo), 0, len, 0x00020000);
    __builtin_amdgcn_raw_ptr_buffer_load_lds(rsC, (LAS void*)(lds + L_O + w * WREG + 16384), 4, tid * 128, 0, 0, 0);
  }
  const int ak = (int)((size_t)kc >> 8) & 3, av = (int)((size_t)vc >> 8) & 3;
  const bool skew = ak == av;
  const int slow = (1 - ak) & 3, nsl = skew ? NSL : 32, nf = skew ? NF : 32;
  const int grp = blockIdx.x & 7;
  int kvh, wg, slowp;
  {
    const int id = grp * 32 + (blockIdx.x >> 3), rem = id & 127, t = rem - nsl;
    const int qd = (t * (skew ? (65536 + NF - 1) / NF : 2048)) >> 16;
    kvh = (slow + (id >> 7) * 4 + (t < 0 ? 0 : 1 + qd)) & 7;
    wg = t < 0 ? rem : t - qd * nf;
    slowp = t < 0;
  }
  const u32x4 qv = *reinterpret_cast<const u32x4*>(q + (w * H + kvh * 4) * D * 2 + lane * 16);
  const int c0 = ctx[r >> 2], c1 = ctx[4 + (r >> 2)];
  const int ckvh = (slow + 4 + grp) & 7, crow = blockIdx.x >> 3;
  const __amdgpu_buffer_rsrc_t rsK = __builtin_amdgcn_make_buffer_rsrc((void*)kc, 0, (unsigned)kv_bytes, 0x00020000);
  const __amdgpu_buffer_rsrc_t rsV = __builtin_amdgcn_make_buffer_rsrc((void*)vc, 0, (unsigned)kv_bytes, 0x00020000);
  const __amdgpu_buffer_rsrc_t rsW = __builtin_amdgcn_make_buffer_rsrc((void*)ws, 0, W_END, 0x00020000);
  const int grow = ckvh * ROWS + crow, myrow = grow * ROW_B, mycell = myrow + CELL;
  const u32x4 e4 = {EMPTY, EMPTY, EMPTY, EMPTY};
  const __amdgpu_buffer_rsrc_t rsO = __builtin_amdgcn_make_buffer_rsrc((void*)out, 0, B * H * D * 2, 0x00020000);
  const int ob = ((crow >> 2) * H + ckvh * 4 + (crow & 3)) * D * 2;
  u32x2 epoch = __builtin_amdgcn_raw_buffer_load_b64(rsW, mycell, 0, AUX_SC1);
  int khead = kvh * 256, cv = (lane & 1) ? nsl : nf;
  const int xr = (r ^ g) * 16;
  int kofs[4], vofs = 4 * g * TOK_BYTES + r * 16;
#pragma unroll
  for (int i = 0; i < 4; ++i) kofs[i] = (4 * i + g) * TOK_BYTES + (xr ^ (64 * i));
  PIN_S(khead); PIN_S(slowp); PIN_S(wg);
  asm volatile("" : "+v"(kofs[0]), "+v"(kofs[1]), "+v"(kofs[2]), "+v"(kofs[3]), "+v"(vofs), "+v"(cv));
  const int nblk = (((craw <= 0 || craw > LK) ? LK : craw) + BS - 1) >> 4;
  int cum = nblk + dpp0<0x111>(nblk); cum += dpp0<0x112>(cum); cum += dpp0<0x114>(cum);
  cum += (r >= 4) ? GAP : 0;
  int mn = 256 - nblk; mn = max(mn, dpp0<0x111>(mn)); mn = max(mn, dpp0<0x112>(mn)); mn = max(mn, dpp0<0x114>(mn));
  int minblk = 256 - __builtin_amdgcn_readlane(mn, 7), total = __builtin_amdgcn_readlane(cum, 7);
  int hi_a = r < 8 ? cum : 0, hi_b = r < 8 ? -r : (r == 8), lo_a = r < 8 ? cum - nblk : 0, lo_b = r < 8 ? 1 - r : 0;
  int btrow = L_BT + (r < 8 ? r : 0) * NB * 4 - 4 * lo_a, bt_b = r < 8 ? 4 * r : 0;
  PIN_S(minblk); PIN_S(total);
  asm volatile("" : "+v"(hi_a), "+v"(hi_b), "+v"(lo_a), "+v"(lo_b), "+v"(btrow), "+v"(bt_b));
  __builtin_amdgcn_sched_barrier(0);
  {
    const unsigned n0 = 4 * ff1(__ballot(ba.x != bz.x)), n1 = 4 * ff1(__ballot(ba.y != bz.y)) + 1, n2 = 4 * ff1(__ballot(ba.z != bz.z)) + 2,
                   n3 = 4 * ff1(__ballot(ba.w != bz.w)) + 3;
    const unsigned ns = min(min(n0, n1), min(n2, n3));
    *reinterpret_cast<unsigned short*>(lds + L_NS + 2 * w) = (unsigned short)ns;
    *reinterpret_cast<i32x4*>(lds + L_BT + tid * 16) = ba;
    if (tid == 0) *reinterpret_cast<int*>(lds + L_CNT) = 0;
  }
  if (w == 0) {
#pragma unroll
    for (int k = 0; k < NEARLY; k += 2) __builtin_amdgcn_raw_buffer_store_b128(e4, rsW, lane * 16, myrow + PAY + k * SLOT_B, AUX_SC1);
    if (lane < CELL / 16) __builtin_amdgcn_raw_buffer_store_b128(e4, rsW, lane * 16, myrow, AUX_SC1);
  }
  BARRIER();
  int NSv;
  {
    const u32x4 n = *reinterpret_cast<const u32x4*>(lds + L_NS);
    const unsigned m = pkmin(pkmin(n.x, n.y), pkmin(n.z, n.w));
    NSv = min((int)min(m & 0xffffu, m >> 16), minblk);
  }
  const int seg_hi = __mul24(hi_b, NSv) + hi_a, seg_lo = __mul24(lo_b, NSv) + lo_a, btadr = __mul24(bt_b, NSv) + btrow;
  struct Plan { int rounds, nw8; float ss, sp; };
  Plan pv;
  {
    const int P = total - 8 * NSv, ns_min = NSv > 0 ? (NSv >> 5) + 1 : 0, np_min = (P >> 5) + 1;
    pv.rounds = ns_min + np_min <= cv ? 1 : ns_min + np_min <= 2 * cv ? 2 : 3;
    const int nc = __mul24(cv, pv.rounds);
    const float fs = (float)NSv, fp = (float)P, fa = fs * (1.f + WNUM * 0.125f);
    const int nw = min(max((int)(fa * (float)nc * __builtin_amdgcn_rcpf(fa + fp) + 0.5f), ns_min), nc - np_min);
    constexpr float UP = 0.125f * (1.f + 1.f / 1048576.f);
    pv.nw8 = 8 * nw; pv.ss = fs * UP * __builtin_amdgcn_rcpf((float)max(nw, 1)); pv.sp = fp * UP * __builtin_amdgcn_rcpf((float)(nc - nw));
  }
  auto plan_of = [&](int odd) {
    return Plan{__builtin_amdgcn_readlane(pv.rounds, odd), __builtin_amdgcn_readlane(pv.nw8, odd),
                __int_as_float(__builtin_amdgcn_readlane(__float_as_int(pv.ss), odd)), __int_as_float(__builtin_amdgcn_readlane(__float_as_int(pv.sp), odd))};
  };
  auto start = [&](const Plan& p, int u) { const bool sh = u < p.nw8; return (sh ? 0 : NSv) + (int)((float)(sh ? u : u - p.nw8) * (sh ? p.ss : p.sp)); };
  auto producers = [&](const Plan& p, int b, int lim) {
    const int lo = __builtin_amdgcn_readlane(seg_lo, b), hi = __builtin_amdgcn_readlane(seg_hi, b);
    const int a = start(p, 8 * lane), e = start(p, 8 * lane + 8);
    int cnt = __builtin_popcountll(__ballot(lane < lim && a < e && (8 * lane < p.nw8 || (a < hi && e > lo))));
    if (lim > 64) {
      const int a2 = start(p, 8 * lane + 512), e2 = start(p, 8 * lane + 520);
      cnt += __builtin_popcountll(__ballot(lane + 64 < lim && a2 < e2 && (8 * lane + 512 < p.nw8 || (a2 < hi && e2 > lo))));
    }
    return cnt;
  };
  const Plan pl = plan_of(slowp);
  int np = 0;
  constexpr float C2 = 0.088388347648318440f * 1.4426950408889634f;

  auto chunk = [&](const int round) __attribute__((always_inline)) {
    const int v = wg * pl.rounds + round;
    const int ws0 = start(pl, 8 * v + w), ws1 = start(pl, 8 * v + w + 1);
    const int f = ws0 + g;
    const bool in = (f >= seg_lo) & (f < min(seg_hi, ws1));
    const int ph = *reinterpret_cast<const int*>(lds + (in ? btadr + 4 * f : L_BT));
    const unsigned long long im = __ballot(in);
    const int sov = (ph << 15) + khead;
    int so[BPW];
    auto count_row = [&]() {
      const Plan cp = plan_of((grp & 3) == 0);
      np = producers(cp, crow >> 2, ((grp & 3) == 0 ? nsl : nf) * cp.rounds);
      const unsigned long long e1 = (((unsigned long long)epoch.y << 32) | epoch.x) + 1ull;
      epoch = u32x2{(unsigned)e1, (unsigned)(e1 >> 32)};
    };
    if (round == 0 && w == 0) {
      count_row();
      for (int k = NEARLY; k < np; k += 2)
        if (k + (lane >> 5) < np) __builtin_amdgcn_raw_buffer_store_b128(e4, rsW, lane * 16, slot_off(grow, k), AUX_SC1);
      asm volatile("s_waitcnt vmcnt(0)" ::: "memory");
      if (lane == 0) __builtin_amdgcn_raw_buffer_store_b64(epoch, rsW, mycell + 8, 0, AUX_SC1);
    }
#pragma unroll
    for (int t = 0; t < BPW; ++t) {
      const unsigned long long mt = im & (0xffffull << (16 * t));
      const int sx = __builtin_amdgcn_readlane(sov, ff1(mt) & 63);
      so[t] = mt ? sx : OOB;
#pragma unroll
      for (int i = 0; i < 4; ++i)
        __builtin_amdgcn_raw_ptr_buffer_load_lds(rsK, (LAS void*)(lds + L_O + w * WREG + t * 4096 + i * 1024), 16, kofs[i], so[t], 0, AUX_NT);
    }
    int jb[BPW], mk[BPW];
    {
      int pk = in ? ((r == 8) ? (f | 0xff0000) : ((f - seg_lo + NSv) | (0x10000 << (r & 7)))) : 0;
      pk |= dpp0<0x111>(pk); pk |= dpp0<0x112>(pk); pk |= dpp0<0x114>(pk); pk |= dpp0<0x118>(pk);
#pragma unroll
      for (int t = 0; t < BPW; ++t) { const int p = __builtin_amdgcn_readlane(pk, 16 * t + 15); jb[t] = p & 0xffff; mk[t] = p >> 16; }
    }
    const int wmask = mk[0] | mk[1] | mk[2] | mk[3];
    const int cl[2] = {(c0 <= 0 || c0 > LK) ? LK : c0, (c1 <= 0 || c1 > LK) ? LK : c1};
    int lim[2][BPW];
#pragma unroll
    for (int rt = 0; rt < 2; ++rt)
#pragma unroll
      for (int t = 0; t < BPW; ++t) { lim[rt][t] = ((mk[t] >> (4 * rt + (r >> 2))) & 1) ? cl[rt] - jb[t] * BS - 4 * g : 0; asm volatile("" : "+v"(lim[rt][t])); }
    const int s0 = start(pl, 8 * v), s1 = start(pl, 8 * v + 8);
    const int cmask = (int)__ballot(lane < 8 && s0 < s1 && (8 * v < pl.nw8 || (s0 < seg_hi && s1 > seg_lo)));
    int tb[2], tr[2];
    {
      int rem = cmask;
      for (int k = 0; k < (w >> 1); ++k) rem &= rem - 1;
#pragma unroll
      for (int it = 0; it < 2; ++it) {
        tb[it] = rem ? __builtin_ctz(rem) : -1;
        tr[it] = rem ? producers(pl, tb[it], v) : 0;
        rem &= rem - 1; rem &= rem - 1; rem &= rem - 1; rem &= rem - 1;
      }
    }
    bool plain[2];
#pragma unroll
    for (int rt = 0; rt < 2; ++rt)
      plain[rt] = __all((min(min(lim[rt][0], lim[rt][1]), min(lim[rt][2], lim[rt][3])) | (((cmask >> (4 * rt + (r >> 2))) & 1) ? 0 : 4)) >= 4);
    if (round == 0) {
      if (w == WAVES - 1) count_row();
      const bool dead = __builtin_amdgcn_readlane(craw, w) <= 0;
      *reinterpret_cast<u32x4*>(lds + L_Q + (w * 4 + g) * QROW + r * 16) = dead ? u32x4{0u, 0u, 0u, 0u} : qv;
    }
    __builtin_amdgcn_sched_barrier(0);
    BARRIER();
    const bool need = (cmask >> ((lane & 31) >> 2)) & 1;
    const int cellofs = need ? (kvh * ROWS + (lane & 31)) * ROW_B + CELL : OOB;
    u32x4 cell = {0u, 0u, 1u, 0u};
    if (w == 0) cell = __builtin_amdgcn_raw_buffer_load_b128(rsW, cellofs, 0, AUX_SC1);
    __builtin_amdgcn_sched_barrier(0);
    u32x4 vr[BPW][4];
#pragma unroll
    for (int t = 0; t < VFIRST; ++t)
#pragma unroll
      for (int i = 0; i < 4; ++i) vr[t][i] = __builtin_amdgcn_raw_buffer_load_b128(rsV, vofs + i * TOK_BYTES, so[t], AUX_NT);
    __builtin_amdgcn_sched_barrier(0);
    asm volatile("s_waitcnt vmcnt(%0)" :: "i"(4 * VFIRST) : "memory");
    u32x4 pf[2][2];
#pragma unroll
    for (int rt = 0; rt < 2; ++rt) {
      float m = -INFINITY, l = 0.f;
      pf[rt][0] = u32x4{0, 0, 0, 0}; pf[rt][1] = u32x4{0, 0, 0, 0};
      if ((wmask >> (4 * rt)) & 0xf) {
        u32x4 qf[4], kf[BPW][4];
        f32x4 s[BPW];
#pragma unroll
        for (int i = 0; i < 4; ++i) {
          qf[i] = *reinterpret_cast<const u32x4*>(lds + L_Q + (16 * rt + r) * QROW + 64 * i + 16 * g);
#pragma unroll
          for (int t = 0; t < BPW; ++t) kf[t][i] = *reinterpret_cast<const u32x4*>(lds + L_O + w * WREG + t * 4096 + r * 256 + (xr ^ (64 * i)));
        }
#pragma unroll
        for (int t = 0; t < BPW; ++t) s[t] = f32x4{0.f, 0.f, 0.f, 0.f};
#pragma unroll
        for (int i = 0; i < 4; ++i)
#pragma unroll
          for (int t = 0; t < BPW; ++t) s[t] = mfma(kf[t][i], qf[i], s[t]);
        if (!plain[rt]) {
#pragma unroll
          for (int t = 0; t < BPW; ++t)
#pragma unroll
            for (int e = 0; e < 4; ++e) s[t][e] = (e < lim[rt][t]) ? s[t][e] : -INFINITY;
        }
#pragma unroll
        for (int t = 0; t < BPW; ++t) m = fmaxf(fmaxf(fmaxf(fmaxf(m, s[t][0]), s[t][1]), s[t][2]), s[t][3]);
        m = xg_max(m) * C2;
        const float ms = (m == -INFINITY) ? 0.f : -m;
#pragma unroll
        for (int t = 0; t < BPW; ++t) {
          f32x4 p;
#pragma unroll
          for (int e = 0; e < 4; ++e) p[e] = __builtin_amdgcn_exp2f(fmaf(s[t][e], C2, ms));
          pf[rt][t >> 1][2 * (t & 1)] = pack_bf16(p[0], p[1]);
          pf[rt][t >> 1][2 * (t & 1) + 1] = pack_bf16(p[2], p[3]);
        }
        const u32x4 ones = {0x3f803f80u, 0x3f803f80u, 0x3f803f80u, 0x3f803f80u};
        l = mfma(ones, pf[rt][1], mfma(ones, pf[rt][0], f32x4{0.f, 0.f, 0.f, 0.f}))[0];
      }
      if (g == 0) {
        *reinterpret_cast<float*>(lds + L_MX + ((16 * rt + r) * WAVES + w) * 4) = m;
        *reinterpret_cast<float*>(lds + L_LS + ((16 * rt + r) * WAVES + w) * 4) = l;
      }
    }
    __builtin_amdgcn_sched_barrier(0);
#pragma unroll
    for (int t = VFIRST; t < BPW; ++t)
#pragma unroll
      for (int i = 0; i < 4; ++i) vr[t][i] = __builtin_amdgcn_raw_buffer_load_b128(rsV, vofs + i * TOK_BYTES, so[t], AUX_NT);
    __builtin_amdgcn_sched_barrier(0);
    asm volatile("s_waitcnt lgkmcnt(0)" ::: "memory");
    if (lane == 0) __hip_atomic_fetch_add(reinterpret_cast<int*>(lds + L_CNT), 1, __ATOMIC_RELAXED, __HIP_MEMORY_SCOPE_WORKGROUP);
    f32x4 acc[2][8];
#pragma unroll
    for (int j = 0; j < 8; ++j) { acc[0][j] = f32x4{0.f, 0.f, 0.f, 0.f}; acc[1][j] = f32x4{0.f, 0.f, 0.f, 0.f}; }
#pragma unroll
    for (int pr = 0; pr < 2; ++pr) {
      u32x4 vt[8];
#pragma unroll
      for (int c = 0; c < 4; ++c) {
#pragma unroll
        for (int j = 0; j < 8; ++j)
          vt[j][c] = __builtin_amdgcn_perm(vr[2 * pr + (c >> 1)][2 * (c & 1) + 1][j >> 1], vr[2 * pr + (c >> 1)][2 * (c & 1)][j >> 1], (j & 1) ? 0x07060302u : 0x05040100u);
        __builtin_amdgcn_sched_barrier(0);
      }
      if (wmask & 0x0f) {
#pragma unroll
        for (int j = 0; j < 8; ++j) acc[0][j] = mfma(vt[j], pf[0][pr], acc[0][j]);
      }
      if (wmask & 0xf0) {
#pragma unroll
        for (int j = 0; j < 8; ++j) acc[1][j] = mfma(vt[j], pf[1][pr], acc[1][j]);
      }
    }
#pragma unroll
    for (int rt = 0; rt < 2; ++rt) {
      if ((cmask >> (4 * rt + (r >> 2))) & 1) {
#pragma unroll
        for (int j = 0; j < 8; ++j) *reinterpret_cast<f32x4*>(lds + L_O + w * WREG + (16 * rt + r) * OROW + (4 * j + g) * 16) = acc[rt][j];
      }
    }
    if (w == 0) {
#pragma unroll 1
      for (unsigned spins = 0; spins < SPIN_CAP; ++spins) {
        const unsigned long long e1 = (((unsigned long long)cell.y << 32) | cell.x) + 1ull;
        const bool ok = !need || (cell.z == (unsigned)e1 && cell.w == (unsigned)(e1 >> 32));
        if (__all(ok)) break;
        __builtin_amdgcn_s_sleep(2);
        asm volatile("" ::: "memory");
        cell = __builtin_amdgcn_raw_buffer_load_b128(rsW, cellofs, 0, AUX_SC1);
      }
    }
    float wt[2][WAVES], Mr[2], Lr[2];
    int oadr[2], gofs[2], gml[2];
    if (tb[0] >= 0) {
      while (__hip_atomic_load(reinterpret_cast<int*>(lds + L_CNT), __ATOMIC_RELAXED, __HIP_MEMORY_SCOPE_WORKGROUP) < WAVES * (round + 1)) __builtin_amdgcn_s_sleep(1);
      asm volatile("" ::: "memory");
    }
#pragma unroll
    for (int it = 0; it < 2; ++it) {
      if (tb[it] >= 0) {
        const int row = 4 * tb[it] + ((tid & 127) >> 5);
        const f32x4 m0 = *reinterpret_cast<const f32x4*>(lds + L_MX + row * WAVES * 4), m1 = *reinterpret_cast<const f32x4*>(lds + L_MX + row * WAVES * 4 + 16);
        const f32x4 l0 = *reinterpret_cast<const f32x4*>(lds + L_LS + row * WAVES * 4), l1 = *reinterpret_cast<const f32x4*>(lds + L_LS + row * WAVES * 4 + 16);
        const float M = fmaxf(fmaxf(fmaxf(fmaxf(fmaxf(fmaxf(fmaxf(m0.x, m0.y), m0.z), m0.w), m1.x), m1.y), m1.z), m1.w), Ms = (M == -INFINITY) ? 0.f : M;
        float L = 0.f;
#pragma unroll
        for (int ww = 0; ww < WAVES; ++ww) {
          wt[it][ww] = __builtin_amdgcn_exp2f((ww < 4 ? m0[ww & 3] : m1[ww & 3]) - Ms);
          L = fmaf(wt[it][ww], ww < 4 ? l0[ww & 3] : l1[ww & 3], L);
          asm volatile("" : "+v"(wt[it][ww]));
        }
        Mr[it] = __uint_as_float(fix(M)); Lr[it] = __uint_as_float(fix(L));
        oadr[it] = L_O + row * OROW + 16 * (tid & 31);
        gml[it] = (kvh * ROWS + row) * ROW_B + tr[it] * 8;
        gofs[it] = slot_off(kvh * ROWS + row, tr[it]) + 16 * (tid & 31);
        asm volatile("" : "+v"(Mr[it]), "+v"(Lr[it]), "+v"(oadr[it]), "+v"(gofs[it]), "+v"(gml[it]));
      }
    }
    __builtin_amdgcn_s_waitcnt(0x0F70);
    BARRIER();
#pragma unroll
    for (int it = 0; it < 2; ++it) {
      if (tb[it] >= 0) {
        f32x4 x[WAVES];
#pragma unroll
        for (int ww = 0; ww < WAVES; ++ww) x[ww] = *reinterpret_cast<const f32x4*>(lds + oadr[it] + ww * WREG);
        if ((tid & 31) == 0) __builtin_amdgcn_raw_buffer_store_b64(u32x2{__float_as_uint(Mr[it]), __float_as_uint(Lr[it])}, rsW, gml[it], 0, AUX_SC1);
        f32x4 o = x[0] * wt[it][0];
#pragma unroll
        for (int ww = 1; ww < WAVES; ++ww) o += x[ww] * wt[it][ww];
        __builtin_amdgcn_raw_buffer_store_b128(u32x4{fix(o.x), fix(o.y), fix(o.z), fix(o.w)}, rsW, gofs[it], 0, AUX_SC1);
      }
    }
  };
  chunk(0);
  const int nrounds = pl.rounds;
  if (__builtin_expect(nrounds > 1, 0)) {
#pragma unroll 1
    for (int round = 1; round < nrounds; ++round) { BARRIER(); chunk(round); }
  }
  if (w == WAVES - 1) {
    const int p4 = lane & 31, hf = lane >> 5;
    float M = -INFINITY, L = 0.f;
    f32x4 o = {0.f, 0.f, 0.f, 0.f};
    struct Set { u32x4 x[PG]; u32x2 ml[PG]; };
    auto fetch = [&](Set& s, int k0) __attribute__((always_inline)) {
      asm volatile("" ::: "memory");
#pragma unroll
      for (int i = 0; i < PG; ++i) {
        const int k = k0 + 2 * i + hf, dead = (np - 1 - k) & OOB;
        s.x[i] = __builtin_amdgcn_raw_buffer_load_b128(rsW, (2 * i * SLOT_B + 16 * lane) | dead, slot_off(grow, k0), AUX_SC1);
        s.ml[i] = __builtin_amdgcn_raw_buffer_load_b64(rsW, (k * 8) | dead, myrow, AUX_SC1);
      }
      asm volatile("" ::: "memory");
    };
    auto complete = [&](const Set& s) __attribute__((always_inline)) {
      unsigned top = 0;
#pragma unroll
      for (int i = 0; i < PG; ++i) top = max(max(max(top, s.x[i].x), max(s.x[i].y, s.x[i].z)), max(max(s.x[i].w, s.ml[i].x), s.ml[i].y));
      return __all(top != EMPTY);
    };
    auto absorb = [&](const Set& s, int k0) __attribute__((always_inline)) {
      float mg = M;
#pragma unroll
      for (int i = 0; i < PG; ++i) mg = fmaxf(mg, (k0 + 2 * i + hf >= np) ? -INFINITY : __uint_as_float(s.ml[i].x));
      mg = xh_max(mg);
      const float ms = (mg == -INFINITY) ? 0.f : mg;
      const float al = __builtin_amdgcn_exp2f(M - ms);
      o *= al; L *= al; M = mg;
#pragma unroll
      for (int i = 0; i < PG; ++i) {
        const float wv = (k0 + 2 * i + hf >= np) ? 0.f : __builtin_amdgcn_exp2f(__uint_as_float(s.ml[i].x) - ms);
        o += __builtin_bit_cast(f32x4, s.x[i]) * wv;
        L = fmaf(wv, __uint_as_float(s.ml[i].y), L);
      }
    };
    auto finish = [&]() __attribute__((always_inline)) {
#pragma unroll
      for (int e = 0; e < 4; ++e) o[e] = xh_sum(o[e]);
      const float inv = __builtin_amdgcn_rcpf(xh_sum(L));
      const unsigned pk = pack_bf16((hf ? o[2] : o[0]) * inv, (hf ? o[3] : o[1]) * inv);
      const int ov = (32 * (p4 & 3) + 16 * hf + (p4 >> 2)) * 2;
      __builtin_amdgcn_raw_buffer_store_b16((unsigned short)pk, rsO, ov, ob, 0);
      __builtin_amdgcn_raw_buffer_store_b16((unsigned short)(pk >> 16), rsO, ov + 16, ob, 0);
      if (lane == 0) __builtin_amdgcn_raw_buffer_store_b64(epoch, rsW, mycell, 0, 0);
    };
    for (int k0 = 0; k0 < np; k0 += 2 * PG) {
      Set sa;
      fetch(sa, k0);
#pragma unroll 1
      for (unsigned spins = 0; spins < SPIN_CAP && !complete(sa); ++spins) { __builtin_amdgcn_s_sleep(1); fetch(sa, k0); }
      absorb(sa, k0);
    }
    finish();
    asm volatile(".Lradix_end:");
  }
}
}

extern "C" void kernel_launch(void* const* d_in, const int* in_sizes, int n_in, void* d_out, int out_size, void* d_ws,
                              size_t ws_size, hipStream_t stream) {
  if (n_in != 5 || out_size != B * H * D || ws_size < (size_t)W_END) return;
  attn_radix<<<dim3(HKV * ROWS), dim3(NTHR), 0, stream>>>((const unsigned char*)d_in[0], (const unsigned char*)d_in[1],
                                                           (const unsigned char*)d_in[2], (const int*)d_in[3],
                                                           (const int*)d_in[4], (unsigned char*)d_out, (unsigned char*)d_ws,
                                                           (unsigned long long)in_sizes[1] * 2ull);
}
```

```cpp
#include <hip/hip_runtime.h>
#include <hip/hip_bf16.h>
#include <stdint.h>

typedef __attribute__((ext_vector_type(8))) __bf16 bf16x8;
typedef __attribute__((ext_vector_type(4))) float f32x4;
typedef __attribute__((ext_vector_type(4))) unsigned u32x4;
typedef __attribute__((ext_vector_type(2))) unsigned u32x2;
typedef __attribute__((ext_vector_type(4))) int i32x4;
typedef __attribute__((ext_vector_type(2))) unsigned short u16x2;
#define LAS __attribute__((address_space(3)))

namespace {
constexpr int B = 8, H = 32, HKV = 8, D = 128, BS = 16, NB = 256, LK = NB * BS;
constexpr int WNUM = 4;
constexpr int NSL = 38;
constexpr int NF = (128 - NSL) / 3;
constexpr int GAP = 3;
constexpr int VFIRST = 2;
constexpr int PG = 7, NEARLY = 2 * PG;
static_assert(NSL + 3 * NF == 128, "four heads share 128 workgroups");
constexpr int WAVES = 8, NTHR = 512;
constexpr int BPW = 4;
constexpr int ROWS = 32;
constexpr int TOK_BYTES = HKV * D * 2;
constexpr int QROW = 272;
constexpr int OROW = 528;
constexpr int WREG = ROWS * OROW;
constexpr int AUX_SC1 = 16, AUX_NT = 2;
constexpr int L_O = 0;
constexpr int L_BT = L_O + WAVES * WREG;
constexpr int L_Q = L_BT + B * NB * 4;
constexpr int L_MX = L_Q + ROWS * QROW;
constexpr int L_LS = L_MX + ROWS * WAVES * 4;
constexpr int L_NS = L_LS + ROWS * WAVES * 4;
constexpr int L_CNT = L_NS + 16;
constexpr int L_END = L_CNT + 16;
constexpr int NSLOT = 96, CELL = 768, PAY = 1024, SLOT_B = 512, ROW_B = PAY + NEARLY * SLOT_B, ROW2_B = (NSLOT - NEARLY) * SLOT_B;
constexpr int W_FAR = HKV * ROWS * ROW_B, W_END = W_FAR + HKV * ROWS * ROW2_B;
__device__ __forceinline__ int slot_off(int grow, int k) { return k < NEARLY ? grow * ROW_B + PAY + k * SLOT_B : W_FAR + grow * ROW2_B + (k - NEARLY) * SLOT_B; }
constexpr unsigned EMPTY = 0xffffffffu;
constexpr unsigned SPIN_CAP = 1u << 20;
constexpr int OOB = (int)0x80000000;

__device__ __forceinline__ float fmax2(float a, float b) { float d; asm("v_max_f32 %0, %1, %2" : "=v"(d) : "v"(a), "v"(b)); return d; }
__device__ __forceinline__ float xg_max(float x) {
  auto s = __builtin_amdgcn_permlane16_swap(__float_as_uint(x), __float_as_uint(x), false, false);
  x = fmax2(__uint_as_float(s[0]), __uint_as_float(s[1]));
  auto t = __builtin_amdgcn_permlane32_swap(__float_as_uint(x), __float_as_uint(x), false, false);
  return fmax2(__uint_as_float(t[0]), __uint_as_float(t[1]));
}
__device__ __forceinline__ float xh_max(float x) {
  auto t = __builtin_amdgcn_permlane32_swap(__float_as_uint(x), __float_as_uint(x), false, false);
  return fmaxf(__uint_as_float(t[0]), __uint_as_float(t[1]));
}
__device__ __forceinline__ float xh_sum(float x) {
  auto t = __builtin_amdgcn_permlane32_swap(__float_as_uint(x), __float_as_uint(x), false, false);
  return __uint_as_float(t[0]) + __uint_as_float(t[1]);
}
__device__ __forceinline__ unsigned pack_bf16(float lo, float hi) {
  typedef __attribute__((ext_vector_type(2))) __bf16 bf16x2;
  bf16x2 v = {(__bf16)lo, (__bf16)hi};
  return __builtin_bit_cast(unsigned, v);
}
__device__ __forceinline__ f32x4 mfma(u32x4 a, u32x4 b, f32x4 c) {
  return __builtin_amdgcn_mfma_f32_16x16x32_bf16(__builtin_bit_cast(bf16x8, a), __builtin_bit_cast(bf16x8, b), c, 0, 0, 0);
}
template <int CTRL> __device__ __forceinline__ int dpp0(int x) {
  return __builtin_amdgcn_update_dpp(0, x, CTRL, 0xf, 0xf, true);
}
__device__ __forceinline__ unsigned fix(float x) { return min(__float_as_uint(x), EMPTY - 1u); }
__device__ __forceinline__ unsigned ff1(unsigned long long m) { unsigned d; asm("s_ff1_i32_b64 %0, %1" : "=s"(d) : "s"(m)); return d; }
__device__ __forceinline__ unsigned pkmin(unsigned a, unsigned b) {
  return __builtin_bit_cast(unsigned, __builtin_elementwise_min(__builtin_bit_cast(u16x2, a), __builtin_bit_cast(u16x2, b)));
}
#define PIN_S(x) do { x = __builtin_amdgcn_readfirstlane(x); asm volatile("" : "+s"(x)); } while (0)
#define BARRIER() asm volatile("s_waitcnt lgkmcnt(0)\n\ts_barrier" ::: "memory")

__global__ void __launch_bounds__(NTHR, 2)
attn_radix(const unsigned char* __restrict__ q, const unsigned char* __restrict__ kc, const unsigned char* __restrict__ vc,
           const int* __restrict__ bt, const int* __restrict__ ctx, unsigned char* __restrict__ out,
           unsigned char* __restrict__ ws, unsigned long long kv_bytes) {
  __shared__ __attribute__((aligned(16))) unsigned char lds[L_END];
  const int tid = threadIdx.x, lane = tid & 63, w = __builtin_amdgcn_readfirstlane(tid >> 6);
  const int g = lane >> 4, r = lane & 15;
  asm volatile("" :: "s"(q), "s"(kc), "s"(vc), "s"(bt), "s"(ctx), "s"(out), "s"(ws), "s"(kv_bytes));
  const int craw = ctx[lane & 7];
  const i32x4 bz = *reinterpret_cast<const i32x4*>(reinterpret_cast<const unsigned char*>(bt) + ((tid + 64) & 511) * 16);
  const i32x4 ba = *reinterpret_cast<const i32x4*>(reinterpret_cast<const unsigned char*>(bt) + tid * 16);
  __builtin_amdgcn_sched_barrier(0);
  {
    unsigned lo, hi, len;
    asm volatile("s_getpc_b64 s[6:7]\n.Lradix_pc:\n\ts_and_b32 %0, s6, 0xffffff00\n\ts_mov_b32 %1, s7\n\ts_mov_b32 %2, .Lradix_end-.Lradix_pc"
                 : "=s"(lo), "=s"(hi), "=s"(len) :: "s6", "s7", "scc");
    const __amdgpu_buffer_rsrc_t rsC = __builtin_amdgcn_make_buffer_rsrc((void*)(((unsigned long long)hi << 32) | lo), 0, len, 0x00020000);
    __builtin_amdgcn_raw_ptr_buffer_load_lds(rsC, (LAS void*)(lds + L_O + w * WREG + 16384), 4, tid * 128, 0, 0, 0);
  }
  const int ak = (int)((size_t)kc >> 8) & 3, av = (int)((size_t)vc >> 8) & 3;
  const bool skew = ak == av;
  const int slow = (1 - ak) & 3, nsl = skew ? NSL : 32, nf = skew ? NF : 32;
  const int grp = blockIdx.x & 7;
  int kvh, wg, slowp;
  {
    const int id = grp * 32 + (blockIdx.x >> 3), rem = id & 127, t = rem - nsl;
    const int qd = (t * (skew ? (65536 + NF - 1) / NF : 2048)) >> 16;
    kvh = (slow + (id >> 7) * 4 + (t < 0 ? 0 : 1 + qd)) & 7;
    wg = t < 0 ? rem : t - qd * nf;
    slowp = (int)((unsigned)t >> 31);
  }
  const u32x4 qv = *reinterpret_cast<const u32x4*>(q + (w * H + kvh * 4) * D * 2 + lane * 16);
  const int ckvh = (slow + 4 + grp) & 7, crow = blockIdx.x >> 3;
  const __amdgpu_buffer_rsrc_t rsK = __builtin_amdgcn_make_buffer_rsrc((void*)kc, 0, (unsigned)kv_bytes, 0x00020000);
  const __amdgpu_buffer_rsrc_t rsV = __builtin_amdgcn_make_buffer_rsrc((void*)vc, 0, (unsigned)kv_bytes, 0x00020000);
  const __amdgpu_buffer_rsrc_t rsW = __builtin_amdgcn_make_buffer_rsrc((void*)ws, 0, W_END, 0x00020000);
  const int grow = ckvh * ROWS + crow, myrow = grow * ROW_B, mycell = myrow + CELL;
  const u32x4 e4 = {EMPTY, EMPTY, EMPTY, EMPTY};
  const __amdgpu_buffer_rsrc_t rsO = __builtin_amdgcn_make_buffer_rsrc((void*)out, 0, B * H * D * 2, 0x00020000);
  const int ob = ((crow >> 2) * H + ckvh * 4 + (crow & 3)) * D * 2;
  u32x2 epoch = __builtin_amdgcn_raw_buffer_load_b64(rsW, (w == 0 || w == WAVES - 1) ? mycell : OOB, 0, AUX_SC1);
  int khead = kvh * 256, nch = slowp ? nsl : nf, odd = 2 * lane - 1;
  unsigned kt0 = (unsigned)(536871424.f * __builtin_amdgcn_rcpf((float)max(lane, 1))), kt1 = (unsigned)(536871424.f * __builtin_amdgcn_rcpf((float)(lane + 64)));
  const int xr = (r ^ g) * 16;
  int kofs[4], vofs = 4 * g * TOK_BYTES + r * 16;
#pragma unroll
  for (int i = 0; i < 4; ++i) kofs[i] = (4 * i + g) * TOK_BYTES + (xr ^ (64 * i));
  PIN_S(khead); PIN_S(nch); PIN_S(wg);
  asm volatile("" : "+v"(kofs[0]), "+v"(kofs[1]), "+v"(kofs[2]), "+v"(kofs[3]), "+v"(vofs), "+v"(odd), "+v"(kt0), "+v"(kt1));
  const int nblk = (((craw <= 0 || craw > LK) ? LK : craw) + BS - 1) >> 4;
  int cum = nblk + dpp0<0x111>(nblk); cum += dpp0<0x112>(cum); cum += dpp0<0x114>(cum);
  cum += (r >= 4) ? GAP : 0;
  int mn = 256 - nblk; mn = max(mn, dpp0<0x111>(mn)); mn = max(mn, dpp0<0x112>(mn)); mn = max(mn, dpp0<0x114>(mn));
  int minblk = 256 - __builtin_amdgcn_readlane(mn, 7), total = __builtin_amdgcn_readlane(cum, 7);
  int hi_a = r < 8 ? cum : 0, hi_b = r < 8 ? -r : (r == 8), lo_a = r < 8 ? cum - nblk : 0, lo_b = r < 8 ? 1 - r : 0;
  int btrow = L_BT + (r < 8 ? r : 0) * NB * 4 - 4 * lo_a, bt_b = r < 8 ? 4 * r : 0;
  PIN_S(minblk); PIN_S(total);
  asm volatile("" : "+v"(hi_a), "+v"(hi_b), "+v"(lo_a), "+v"(lo_b), "+v"(btrow), "+v"(bt_b));
  __builtin_amdgcn_sched_barrier(0);
  {
    const unsigned n0 = 4 * ff1(__ballot(ba.x != bz.x)), n1 = 4 * ff1(__ballot(ba.y != bz.y)) + 1, n2 = 4 * ff1(__ballot(ba.z != bz.z)) + 2,
                   n3 = 4 * ff1(__ballot(ba.w != bz.w)) + 3;
    const unsigned ns = min(min(n0, n1), min(n2, n3));
    *reinterpret_cast<unsigned short*>(lds + L_NS + 2 * w) = (unsigned short)ns;
    *reinterpret_cast<i32x4*>(lds + L_BT + tid * 16) = ba;
    if (tid == 0) *reinterpret_cast<int*>(lds + L_CNT) = 0;
  }
  if (w == 0) {
#pragma unroll
    for (int k = 0; k < NEARLY; k += 2) __builtin_amdgcn_raw_buffer_store_b128(e4, rsW, lane * 16, myrow + PAY + k * SLOT_B, AUX_SC1);
    if (lane < CELL / 16) __builtin_amdgcn_raw_buffer_store_b128(e4, rsW, lane * 16, myrow, AUX_SC1);
  }
  BARRIER();
  int NS;
  {
    const u32x4 n = *reinterpret_cast<const u32x4*>(lds + L_NS);
    const unsigned m = __builtin_amdgcn_readfirstlane(pkmin(pkmin(n.x, n.y), pkmin(n.z, n.w)));
    NS = min((int)min(m & 0xffffu, m >> 16), minblk);
  }
  const int P = total - 8 * NS;
  const int seg_hi = __mul24(hi_b, NS) + hi_a, seg_lo = __mul24(lo_b, NS) + lo_a, btadr = __mul24(bt_b, NS) + btrow;
  struct Plan { int rounds, nw8; unsigned ks, kp; };
  auto plan_of = [&](int c) {
    const int ns_min = NS > 0 ? (NS >> 5) + 1 : 0, np_min = (P >> 5) + 1;
    const int rounds = ns_min + np_min <= c ? 1 : ns_min + np_min <= 2 * c ? 2 : 3, nc = c * rounds, a8 = (8 + WNUM) * NS;
    const int nw = min(max(__builtin_popcountll(__ballot(__mul24(odd, a8 + 8 * P) <= 2 * nc * a8) >> 1), ns_min), nc - np_min);
    const unsigned k0 = __builtin_amdgcn_readlane(kt0, (nc - nw) & 63), k1 = __builtin_amdgcn_readlane(kt1, (nc - nw) & 63);
    return Plan{rounds, 8 * nw, (unsigned)__builtin_amdgcn_readlane(kt0, nw), nc - nw < 64 ? k0 : k1};
  };
  auto start = [&](const Plan& p, int u) {
    const bool sh = u < p.nw8;
    return (sh ? 0 : NS) + (int)__umulhi((unsigned)(sh ? u * NS : (u - p.nw8) * P), sh ? p.ks : p.kp);
  };
  auto producers = [&](const Plan& p, int b, int lim) {
    const int lo = __builtin_amdgcn_readlane(seg_lo, b), hi = __builtin_amdgcn_readlane(seg_hi, b);
    const int a = start(p, 8 * lane), e = start(p, 8 * lane + 8);
    int cnt = __builtin_popcountll(__ballot(lane < lim && a < e && (8 * lane < p.nw8 || (a < hi && e > lo))));
    if (lim > 64) {
      const int a2 = start(p, 8 * lane + 512), e2 = start(p, 8 * lane + 520);
      cnt += __builtin_popcountll(__ballot(lane + 64 < lim && a2 < e2 && (8 * lane + 512 < p.nw8 || (a2 < hi && e2 > lo))));
    }
    return cnt;
  };
  const Plan pl = plan_of(nch);
  int np = 0;
  constexpr float C2 = 0.088388347648318440f * 1.4426950408889634f;

  auto chunk = [&](const int round) __attribute__((always_inline)) {
    const int v = wg * pl.rounds + round;
    int ws0, ws1;
    {
      const bool sh = 8 * v < pl.nw8;
      const int n = sh ? NS : P, base = sh ? 0 : NS;
      const unsigned k = sh ? pl.ks : pl.kp, t0 = (unsigned)((8 * v + w - (sh ? 0 : pl.nw8)) * n);
      ws0 = base + (int)__umulhi(t0, k); ws1 = base + (int)__umulhi(t0 + n, k);
    }
    const int f = ws0 + g;
    const bool in = (f >= seg_lo) & (f < min(seg_hi, ws1));
    const int ph = *reinterpret_cast<const int*>(lds + (in ? btadr + 4 * f : L_BT));
    const unsigned long long im = __ballot(in);
    const int sov = (ph << 15) + khead;
    int so[BPW];
    auto count_row = [&]() {
      const int cc = (grp & 3) == 0 ? nsl : nf;
      const Plan cp = plan_of(cc);
      np = producers(cp, crow >> 2, cc * cp.rounds);
      const unsigned long long e1 = (((unsigned long long)epoch.y << 32) | epoch.x) + 1ull;
      epoch = u32x2{(unsigned)e1, (unsigned)(e1 >> 32)};
    };
    if (round == 0 && w == 0) {
      count_row();
      for (int k = NEARLY; k < np; k += 2)
        if (k + (lane >> 5) < np) __builtin_amdgcn_raw_buffer_store_b128(e4, rsW, lane * 16, slot_off(grow, k), AUX_SC1);
      asm volatile("s_waitcnt vmcnt(0)" ::: "memory");
      if (lane == 0) __builtin_amdgcn_raw_buffer_store_b64(epoch, rsW, mycell + 8, 0, AUX_SC1);
    }
#pragma unroll
    for (int t = 0; t < BPW; ++t) {
      const unsigned long long mt = im & (0xffffull << (16 * t));
      const int sx = __builtin_amdgcn_readlane(sov, ff1(mt) & 63);
      so[t] = mt ? sx : OOB;
#pragma unroll
      for (int i = 0; i < 4; ++i)
        __builtin_amdgcn_raw_ptr_buffer_load_lds(rsK, (LAS void*)(lds + L_O + w * WREG + t * 4096 + i * 1024), 16, kofs[i], so[t], 0, AUX_NT);
    }
    int jb[BPW], mk[BPW];
    {
      int pk = in ? ((r == 8) ? (f | 0xff0000) : ((f - seg_lo + NS) | (0x10000 << (r & 7)))) : 0;
      pk |= dpp0<0x111>(pk); pk |= dpp0<0x112>(pk); pk |= dpp0<0x114>(pk); pk |= dpp0<0x118>(pk);
#pragma unroll
      for (int t = 0; t < BPW; ++t) { const int p = __builtin_amdgcn_readlane(pk, 16 * t + 15); jb[t] = p & 0xffff; mk[t] = p >> 16; }
    }
    const int wmask = mk[0] | mk[1] | mk[2] | mk[3];
    const int c0 = __builtin_amdgcn_ds_bpermute(r & 12, craw), c1 = __builtin_amdgcn_ds_bpermute(16 + (r & 12), craw);
    const int cl[2] = {(c0 <= 0 || c0 > LK) ? LK : c0, (c1 <= 0 || c1 > LK) ? LK : c1};
    int lim[2][BPW];
#pragma unroll
    for (int rt = 0; rt < 2; ++rt)
#pragma unroll
      for (int t = 0; t < BPW; ++t) { lim[rt][t] = ((mk[t] >> (4 * rt + (r >> 2))) & 1) ? cl[rt] - jb[t] * BS - 4 * g : 0; asm volatile("" : "+v"(lim[rt][t])); }
    const int s0 = start(pl, 8 * v), s1 = start(pl, 8 * v + 8);
    const int cmask = (int)__ballot(lane < 8 && s0 < s1 && (8 * v < pl.nw8 || (s0 < seg_hi && s1 > seg_lo)));
    int tb[2], tr[2];
    {
      int rem = cmask;
      for (int k = 0; k < (w >> 1); ++k) rem &= rem - 1;
#pragma unroll
      for (int it = 0; it < 2; ++it) {
        tb[it] = rem ? __builtin_ctz(rem) : -1;
        tr[it] = rem ? producers(pl, tb[it], v) : 0;
        rem &= rem - 1; rem &= rem - 1; rem &= rem - 1; rem &= rem - 1;
      }
    }
    bool plain[2];
#pragma unroll
    for (int rt = 0; rt < 2; ++rt)
      plain[rt] = __all((min(min(lim[rt][0], lim[rt][1]), min(lim[rt][2], lim[rt][3])) | (((cmask >> (4 * rt + (r >> 2))) & 1) ? 0 : 4)) >= 4);
    if (round == 0) {
      if (w == WAVES - 1) count_row();
      const bool dead = __builtin_amdgcn_readlane(craw, w) <= 0;
      *reinterpret_cast<u32x4*>(lds + L_Q + (w * 4 + g) * QROW + r * 16) = dead ? u32x4{0u, 0u, 0u, 0u} : qv;
    }
    __builtin_amdgcn_sched_barrier(0);
    BARRIER();
    const bool need = (cmask >> ((lane & 31) >> 2)) & 1;
    const int cellofs = need ? (kvh * ROWS + (lane & 31)) * ROW_B + CELL : OOB;
    u32x4 cell = {0u, 0u, 1u, 0u};
    if (w == 0) cell = __builtin_amdgcn_raw_buffer_load_b128(rsW, cellofs, 0, AUX_SC1);
    __builtin_amdgcn_sched_barrier(0);
    u32x4 vr[BPW][4];
#pragma unroll
    for (int t = 0; t < VFIRST; ++t)
#pragma unroll
      for (int i = 0; i < 4; ++i) vr[t][i] = __builtin_amdgcn_raw_buffer_load_b128(rsV, vofs + i * TOK_BYTES, so[t], AUX_NT);
    __builtin_amdgcn_sched_barrier(0);
    asm volatile("s_waitcnt vmcnt(%0)" :: "i"(4 * VFIRST) : "memory");
    u32x4 pf[2][2];
#pragma unroll
    for (int rt = 0; rt < 2; ++rt) {
      float m = -INFINITY, l = 0.f;
      pf[rt][0] = u32x4{0, 0, 0, 0}; pf[rt][1] = u32x4{0, 0, 0, 0};
      if ((wmask >> (4 * rt)) & 0xf) {
        u32x4 qf[4], kf[BPW][4];
        f32x4 s[BPW];
#pragma unroll
        for (int i = 0; i < 4; ++i) {
          qf[i] = *reinterpret_cast<const u32x4*>(lds + L_Q + (16 * rt + r) * QROW + 64 * i + 16 * g);
#pragma unroll
          for (int t = 0; t < BPW; ++t) kf[t][i] = *reinterpret_cast<const u32x4*>(lds + L_O + w * WREG + t * 4096 + r * 256 + (xr ^ (64 * i)));
        }
#pragma unroll
        for (int t = 0; t < BPW; ++t) s[t] = f32x4{0.f, 0.f, 0.f, 0.f};
#pragma unroll
        for (int i = 0; i < 4; ++i)
#pragma unroll
          for (int t = 0; t < BPW; ++t) s[t] = mfma(kf[t][i], qf[i], s[t]);
        if (!plain[rt]) {
#pragma unroll
          for (int t = 0; t < BPW; ++t)
#pragma unroll
            for (int e = 0; e < 4; ++e) s[t][e] = (e < lim[rt][t]) ? s[t][e] : -INFINITY;
        }
#pragma unroll
        for (int t = 0; t < BPW; ++t) m = fmaxf(fmaxf(fmaxf(fmaxf(m, s[t][0]), s[t][1]), s[t][2]), s[t][3]);
        m = xg_max(m) * C2;
        const float ms = (m == -INFINITY) ? 0.f : -m;
#pragma unroll
        for (int t = 0; t < BPW; ++t) {
          f32x4 p;
#pragma unroll
          for (int e = 0; e < 4; ++e) p[e] = __builtin_amdgcn_exp2f(fmaf(s[t][e], C2, ms));
          pf[rt][t >> 1][2 * (t & 1)] = pack_bf16(p[0], p[1]);
          pf[rt][t >> 1][2 * (t & 1) + 1] = pack_bf16(p[2], p[3]);
        }
        const u32x4 ones = {0x3f803f80u, 0x3f803f80u, 0x3f803f80u, 0x3f803f80u};
        l = mfma(ones, pf[rt][1], mfma(ones, pf[rt][0], f32x4{0.f, 0.f, 0.f, 0.f}))[0];
      }
      if (g == 0) {
        *reinterpret_cast<float*>(lds + L_MX + ((16 * rt + r) * WAVES + w) * 4) = m;
        *reinterpret_cast<float*>(lds + L_LS + ((16 * rt + r) * WAVES + w) * 4) = l;
      }
    }
    __builtin_amdgcn_sched_barrier(0);
#pragma unroll
    for (int t = VFIRST; t < BPW; ++t)
#pragma unroll
      for (int i = 0; i < 4; ++i) vr[t][i] = __builtin_amdgcn_raw_buffer_load_b128(rsV, vofs + i * TOK_BYTES, so[t], AUX_NT);
    __builtin_amdgcn_sched_barrier(0);
    asm volatile("s_waitcnt lgkmcnt(0)" ::: "memory");
    if (lane == 0) __hip_atomic_fetch_add(reinterpret_cast<int*>(lds + L_CNT), 1, __ATOMIC_RELAXED, __HIP_MEMORY_SCOPE_WORKGROUP);
    f32x4 acc[2][8];
#pragma unroll
    for (int j = 0; j < 8; ++j) { acc[0][j] = f32x4{0.f, 0.f, 0.f, 0.f}; acc[1][j] = f32x4{0.f, 0.f, 0.f, 0.f}; }
#pragma unroll
    for (int pr = 0; pr < 2; ++pr) {
      u32x4 vt[8];
#pragma unroll
      for (int c = 0; c < 4; ++c) {
#pragma unroll
        for (int j = 0; j < 8; ++j)
          vt[j][c] = __builtin_amdgcn_perm(vr[2 * pr + (c >> 1)][2 * (c & 1) + 1][j >> 1], vr[2 * pr + (c >> 1)][2 * (c & 1)][j >> 1], (j & 1) ? 0x07060302u : 0x05040100u);
        __builtin_amdgcn_sched_barrier(0);
      }
      if (wmask & 0x0f) {
#pragma unroll
        for (int j = 0; j < 8; ++j) acc[0][j] = mfma(vt[j], pf[0][pr], acc[0][j]);
      }
      if (wmask & 0xf0) {
#pragma unroll
        for (int j = 0; j < 8; ++j) acc[1][j] = mfma(vt[j], pf[1][pr], acc[1][j]);
      }
    }
#pragma unroll
    for (int rt = 0; rt < 2; ++rt) {
      if ((cmask >> (4 * rt + (r >> 2))) & 1) {
#pragma unroll
        for (int j = 0; j < 8; ++j) *reinterpret_cast<f32x4*>(lds + L_O + w * WREG + (16 * rt + r) * OROW + (4 * j + g) * 16) = acc[rt][j];
      }
    }
    if (w == 0) {
#pragma unroll 1
      for (unsigned spins = 0; spins < SPIN_CAP; ++spins) {
        const unsigned long long e1 = (((unsigned long long)cell.y << 32) | cell.x) + 1ull;
        const bool ok = !need || (cell.z == (unsigned)e1 && cell.w == (unsigned)(e1 >> 32));
        if (__all(ok)) break;
        __builtin_amdgcn_s_sleep(2);
        asm volatile("" ::: "memory");
        cell = __builtin_amdgcn_raw_buffer_load_b128(rsW, cellofs, 0, AUX_SC1);
      }
    }
    float wt[2][WAVES], Mr[2], Lr[2];
    int oadr[2], gofs[2], gml[2];
    if (tb[0] >= 0) {
      while (__hip_atomic_load(reinterpret_cast<int*>(lds + L_CNT), __ATOMIC_RELAXED, __HIP_MEMORY_SCOPE_WORKGROUP) < WAVES * (round + 1)) __builtin_amdgcn_s_sleep(1);
      asm volatile("" ::: "memory");
    }
#pragma unroll
    for (int it = 0; it < 2; ++it) {
      if (tb[it] >= 0) {
        const int row = 4 * tb[it] + ((tid & 127) >> 5);
        const f32x4 m0 = *reinterpret_cast<const f32x4*>(lds + L_MX + row * WAVES * 4), m1 = *reinterpret_cast<const f32x4*>(lds + L_MX + row * WAVES * 4 + 16);
        const f32x4 l0 = *reinterpret_cast<const f32x4*>(lds + L_LS + row * WAVES * 4), l1 = *reinterpret_cast<const f32x4*>(lds + L_LS + row * WAVES * 4 + 16);
        const float M = fmaxf(fmaxf(fmaxf(fmaxf(fmaxf(fmaxf(fmaxf(m0.x, m0.y), m0.z), m0.w), m1.x), m1.y), m1.z), m1.w), Ms = (M == -INFINITY) ? 0.f : M;
        float L = 0.f;
#pragma unroll
        for (int ww = 0; ww < WAVES; ++ww) {
          wt[it][ww] = __builtin_amdgcn_exp2f((ww < 4 ? m0[ww & 3] : m1[ww & 3]) - Ms);
          L = fmaf(wt[it][ww], ww < 4 ? l0[ww & 3] : l1[ww & 3], L);
          asm volatile("" : "+v"(wt[it][ww]));
        }
        Mr[it] = __uint_as_float(fix(M)); Lr[it] = __uint_as_float(fix(L));
        oadr[it] = L_O + row * OROW + 16 * (tid & 31);
        gml[it] = (kvh * ROWS + row) * ROW_B + tr[it] * 8;
        gofs[it] = slot_off(kvh * ROWS + row, tr[it]) + 16 * (tid & 31);
        asm volatile("" : "+v"(Mr[it]), "+v"(Lr[it]), "+v"(oadr[it]), "+v"(gofs[it]), "+v"(gml[it]));
      }
    }
    __builtin_amdgcn_s_waitcnt(0x0F70);
    BARRIER();
#pragma unroll
    for (int it = 0; it < 2; ++it) {
      if (tb[it] >= 0) {
        f32x4 x[WAVES];
#pragma unroll
        for (int ww = 0; ww < WAVES; ++ww) x[ww] = *reinterpret_cast<const f32x4*>(lds + oadr[it] + ww * WREG);
        if ((tid & 31) == 0) __builtin_amdgcn_raw_buffer_store_b64(u32x2{__float_as_uint(Mr[it]), __float_as_uint(Lr[it])}, rsW, gml[it], 0, AUX_SC1);
        f32x4 o = x[0] * wt[it][0];
#pragma unroll
        for (int ww = 1; ww < WAVES; ++ww) o += x[ww] * wt[it][ww];
        __builtin_amdgcn_raw_buffer_store_b128(u32x4{fix(o.x), fix(o.y), fix(o.z), fix(o.w)}, rsW, gofs[it], 0, AUX_SC1);
      }
    }
  };
  chunk(0);
  const int nrounds = pl.rounds;
  if (__builtin_expect(nrounds > 1, 0)) {
#pragma unroll 1
    for (int round = 1; round < nrounds; ++round) { BARRIER(); chunk(round); }
  }
  if (w == WAVES - 1) {
    const int p4 = lane & 31, hf = lane >> 5;
    float M = -INFINITY, L = 0.f;
    f32x4 o = {0.f, 0.f, 0.f, 0.f};
    struct Set { u32x4 x[PG]; u32x2 ml[PG]; };
    auto fetch = [&](Set& s, int k0) __attribute__((always_inline)) {
      asm volatile("" ::: "memory");
#pragma unroll
      for (int i = 0; i < PG; ++i) {
        const int k = k0 + 2 * i + hf, dead = (np - 1 - k) & OOB;
        s.x[i] = __builtin_amdgcn_raw_buffer_load_b128(rsW, (2 * i * SLOT_B + 16 * lane) | dead, slot_off(grow, k0), AUX_SC1);
        s.ml[i] = __builtin_amdgcn_raw_buffer_load_b64(rsW, (k * 8) | dead, myrow, AUX_SC1);
      }
      asm volatile("" ::: "memory");
    };
    auto complete = [&](const Set& s) __attribute__((always_inline)) {
      unsigned top = 0;
#pragma unroll
      for (int i = 0; i < PG; ++i) top = max(max(max(top, s.x[i].x), max(s.x[i].y, s.x[i].z)), max(max(s.x[i].w, s.ml[i].x), s.ml[i].y));
      return __all(top != EMPTY);
    };
    auto absorb = [&](const Set& s, int k0) __attribute__((always_inline)) {
      float mg = M;
#pragma unroll
      for (int i = 0; i < PG; ++i) mg = fmaxf(mg, (k0 + 2 * i + hf >= np) ? -INFINITY : __uint_as_float(s.ml[i].x));
      mg = xh_max(mg);
      const float ms = (mg == -INFINITY) ? 0.f : mg;
      const float al = __builtin_amdgcn_exp2f(M - ms);
      o *= al; L *= al; M = mg;
#pragma unroll
      for (int i = 0; i < PG; ++i) {
        const float wv = (k0 + 2 * i + hf >= np) ? 0.f : __builtin_amdgcn_exp2f(__uint_as_float(s.ml[i].x) - ms);
        o += __builtin_bit_cast(f32x4, s.x[i]) * wv;
        L = fmaf(wv, __uint_as_float(s.ml[i].y), L);
      }
    };
    auto finish = [&]() __attribute__((always_inline)) {
#pragma unroll
      for (int e = 0; e < 4; ++e) o[e] = xh_sum(o[e]);
      const float inv = __builtin_amdgcn_rcpf(xh_sum(L));
      const unsigned pk = pack_bf16((hf ? o[2] : o[0]) * inv, (hf ? o[3] : o[1]) * inv);
      const int ov = (32 * (p4 & 3) + 16 * hf + (p4 >> 2)) * 2;
      __builtin_amdgcn_raw_buffer_store_b16((unsigned short)pk, rsO, ov, ob, 0);
      __builtin_amdgcn_raw_buffer_store_b16((unsigned short)(pk >> 16), rsO, ov + 16, ob, 0);
      if (lane == 0) __builtin_amdgcn_raw_buffer_store_b64(epoch, rsW, mycell, 0, 0);
    };
    for (int k0 = 0; k0 < np; k0 += 2 * PG) {
      Set sa;
      fetch(sa, k0);
#pragma unroll 1
      for (unsigned spins = 0; spins < SPIN_CAP && !complete(sa); ++spins) { __builtin_amdgcn_s_sleep(1); fetch(sa, k0); }
      absorb(sa, k0);
    }
    finish();
    asm volatile(".Lradix_end:");
  }
}
}

extern "C" void kernel_launch(void* const* d_in, const int* in_sizes, int n_in, void* d_out, int out_size, void* d_ws,
                              size_t ws_size, hipStream_t stream) {
  if (n_in != 5 || out_size != B * H * D || ws_size < (size_t)W_END) return;
  attn_radix<<<dim3(HKV * ROWS), dim3(NTHR), 0, stream>>>((const unsigned char*)d_in[0], (const unsigned char*)d_in[1],
                                                           (const unsigned char*)d_in[2], (const int*)d_in[3],
                                                           (const int*)d_in[4], (unsigned char*)d_out, (unsigned char*)d_ws,
                                                           (unsigned long long)in_sizes[1] * 2ull);
}
```
